```python
import torch

def _softmax(x, axis=-1):
    return torch.softmax(x, dim=axis)
def _gelu(x):
    return torch.nn.functional.gelu(x, approximate="tanh")
def _silu(x):
    return torch.nn.functional.silu(x)
def _sigmoid(x):
    return torch.sigmoid(x)
def _layernorm(x, axis=-1, eps=1e-5):
    m = x.mean(dim=axis, keepdim=True)
    v = x.var(dim=axis, keepdim=True, unbiased=False)
    return (x - m) / torch.sqrt(v + eps)
def _scatter_add(tgt, idx, src, axis=0):
    return tgt.clone().index_add_(axis, idx, src)
def _segment_sum(src, seg, n):
    out = torch.zeros((n,) + src.shape[1:], dtype=src.dtype)
    return out.index_add_(0, seg, src)
def _pad(x, pw):
    flat = tuple(v for pair in reversed(pw) for v in pair)
    return torch.nn.functional.pad(x, flat)

kernel_name = 'sequential_preLN__gqa_block_cached__dense_activation_squared_relu__decode'

def setup_inputs():

    _w = (lambda a: (lambda t: t.to(torch.bfloat16) if t.is_floating_point() else t)(torch.as_tensor(a)))
    def _A_setup():
        B, Sq, D = 1, 1, 512
        Hq, Hkv, Sk, Dh = 32, 8, 65536, 128
        assert Hq % Hkv == 0, (Hq, Hkv)
        return {
            "x": _w((lambda *s: torch.randn(*s, dtype=torch.bfloat16) * 0.05)(B, Sq, D)),
            "k_cache": _w((lambda *s: torch.randn(*s, dtype=torch.bfloat16) * 3.2)(B, Hkv, Sk, Dh)),
            "v_cache": _w((lambda *s: torch.randn(*s, dtype=torch.bfloat16) * 0.4)(B, Hkv, Sk, Dh)),
            "wq": _w((lambda *s: torch.randn(*s, dtype=torch.bfloat16) * 3.2)(D, Hq * Dh) * (D ** -0.5)),
            "wo": _w((lambda *s: torch.randn(*s, dtype=torch.bfloat16) * 0.4)(Hq * Dh, D) * ((Hq * Dh) ** -0.5)),
        }
    _A_in = _A_setup()

    def _B_setup():
        B, S, D, F = 1, 1, 512, 1792
        return {
            "x": _w((lambda *s: torch.randn(*s, dtype=torch.bfloat16) * 0.05)(B, S, D)),
            "w1": _w((lambda *s: torch.randn(*s, dtype=torch.bfloat16) * 0.05)(D, F)),
            "b1": _w((lambda *s: torch.randn(*s, dtype=torch.bfloat16) * 0.8)(F)),
            "w2": _w((lambda *s: torch.randn(*s, dtype=torch.bfloat16) * 0.05)(F, D)),
            "b2": _w((lambda *s: torch.randn(*s, dtype=torch.bfloat16) * 3.2)(D)),
        }
    _B_in = _B_setup()

    return {
        "A_x": _A_in["x"],
        "A_k_cache": _A_in["k_cache"],
        "A_v_cache": _A_in["v_cache"],
        "A_wq": _A_in["wq"],
        "A_wo": _A_in["wo"],
        "B_x": _B_in["x"],
        "B_w1": _B_in["w1"],
        "B_b1": _B_in["b1"],
        "B_w2": _B_in["w2"],
        "B_b2": _B_in["b2"],
    }

def reference(A_x, A_k_cache, A_v_cache, A_wq, A_wo, B_x, B_w1, B_b1, B_w2, B_b2):
    def _A_ref(x, k_cache, v_cache, wq, wo):
        B, Sq, D = x.shape
        Hq, Dh = 32, 128
        Hkv = k_cache.shape[1]
        rep = Hq // Hkv
        q = (lambda x, *d: x.permute(*d))((x @ wq).reshape(B, Sq, Hq, Dh), 0, 2, 1, 3)
        k = (lambda x, n, axis: torch.repeat_interleave(x, n, dim=axis))(k_cache, rep, axis=1)
        v = (lambda x, n, axis: torch.repeat_interleave(x, n, dim=axis))(v_cache, rep, axis=1)
        scores = torch.einsum("bhqd,bhkd->bhqk", q, k) * (Dh ** -0.5)
        p = _softmax(scores, axis=-1)
        o = torch.einsum("bhqk,bhkd->bhqd", p, v)
        return (lambda x, *d: x.permute(*d))(o, 0, 2, 1, 3).reshape(B, Sq, Hq * Dh) @ wo

    def _B_ref(x, w1, b1, w2, b2):
        h = torch.einsum("bsd,df->bsf", x, w1) + b1
        zero = torch.zeros_like(h)
        r = torch.where(h > zero, h, zero)
        h = r * r
        out = torch.einsum("bsf,fd->bsd", h, w2) + b2
        return out

    x = A_x
    h = x + _A_ref(_layernorm(x, axis=-1), A_k_cache, A_v_cache, A_wq, A_wo)
    h2 = h + _B_ref(_layernorm(h, axis=-1), B_w1, B_b1, B_w2, B_b2)
    return h2
```

```cpp
#include <hip/hip_runtime.h>
#include <hip/hip_bf16.h>
#include <stdint.h>

typedef unsigned short u16;
typedef unsigned int u32;
typedef u32 u32x4 __attribute__((ext_vector_type(4)));
typedef float f32x4 __attribute__((ext_vector_type(4)));
typedef __bf16 bf16x8 __attribute__((ext_vector_type(8)));

constexpr int D_MODEL = 512, HQ = 32, HKV = 8, SK = 65536, DH = 128, FF = 1792;
constexpr int NSPLIT = 32;
constexpr int KEYS_PER_WG = SK / NSPLIT;
constexpr int ATT_WAVES = 8;
constexpr int TILES_PER_WAVE = KEYS_PER_WG / (32 * ATT_WAVES);
constexpr int NC = 28;
constexpr float SCALE = 0.08838834764831845f;
constexpr float L2E = 1.4426950408889634f;

constexpr size_t WS_Q = 0;
constexpr size_t WS_PM = 8192;
constexpr size_t WS_PL = WS_PM + HQ * NSPLIT * 4;
constexpr size_t WS_PO = WS_PL + HQ * NSPLIT * 4;
constexpr size_t WS_Y = WS_PO + (size_t)HQ * NSPLIT * DH * 4;
constexpr size_t WS_Z = WS_Y + HQ * D_MODEL * 4;
constexpr size_t WS_H = WS_Z + NC * D_MODEL * 4;

__device__ __forceinline__ float rbf(float x) { return (float)(__bf16)x; }
__device__ __forceinline__ float bf_lo(u32 w) { return __builtin_bit_cast(float, w << 16); }
__device__ __forceinline__ float bf_hi(u32 w) { return __builtin_bit_cast(float, w & 0xffff0000u); }
__device__ __forceinline__ float bf2f(u16 v) { return __builtin_bit_cast(float, (u32)v << 16); }
__device__ __forceinline__ u16 f2bf(float x) { return __builtin_bit_cast(u16, (__bf16)x); }
__device__ __forceinline__ u32 pack_bf(float lo, float hi) { return (u32)f2bf(lo) | ((u32)f2bf(hi) << 16); }

__device__ __forceinline__ f32x4 mfma16(u32x4 a, u32x4 b, f32x4 c) {
  return __builtin_amdgcn_mfma_f32_16x16x32_bf16(__builtin_bit_cast(bf16x8, a), __builtin_bit_cast(bf16x8, b), c, 0, 0, 0);
}
__device__ __forceinline__ float xrow16_max(float x) {
  auto s = __builtin_amdgcn_permlane16_swap(__float_as_uint(x), __float_as_uint(x), false, false);
  x = fmaxf(__uint_as_float(s[0]), __uint_as_float(s[1]));
  auto t = __builtin_amdgcn_permlane32_swap(__float_as_uint(x), __float_as_uint(x), false, false);
  return fmaxf(__uint_as_float(t[0]), __uint_as_float(t[1]));
}
__device__ __forceinline__ float xrow16_sum(float x) {
  auto s = __builtin_amdgcn_permlane16_swap(__float_as_uint(x), __float_as_uint(x), false, false);
  x = __uint_as_float(s[0]) + __uint_as_float(s[1]);
  auto t = __builtin_amdgcn_permlane32_swap(__float_as_uint(x), __float_as_uint(x), false, false);
  return __uint_as_float(t[0]) + __uint_as_float(t[1]);
}
__device__ __forceinline__ float wave_sum(float v) {
#pragma unroll
  for (int o = 32; o >= 1; o >>= 1) v += __shfl_xor(v, o);
  return v;
}

__device__ __forceinline__ float layernorm512(float xv, float* red, int tid) {
  float s = wave_sum(xv);
  if ((tid & 63) == 0) red[tid >> 6] = s;
  __syncthreads();
  float tot = 0.f;
#pragma unroll
  for (int w = 0; w < 8; ++w) tot += red[w];
  const float mean = tot * (1.0f / 512.0f);
  const float dv = xv - mean;
  float s2 = wave_sum(dv * dv);
  if ((tid & 63) == 0) red[8 + (tid >> 6)] = s2;
  __syncthreads();
  float tot2 = 0.f;
#pragma unroll
  for (int w = 0; w < 8; ++w) tot2 += red[8 + w];
  const float var = rbf(tot2 * (1.0f / 512.0f));
  const float mb = rbf(mean);
  const float den = rbf(sqrtf(rbf(var + 1e-5f)));
  return rbf(rbf(xv - mb) / den);
}

__global__ __launch_bounds__(512) void k_qproj(const u16* __restrict__ x, const u16* __restrict__ wq, u16* __restrict__ qout) {
  __shared__ float red[16];
  __shared__ float lnx[512];
  __shared__ float part[8][16];
  const int tid = threadIdx.x, lane = tid & 63, wave = tid >> 6;
  const int b = blockIdx.x, kvh = b & 7, sp = b >> 3;
  const int col0 = kvh * 512 + sp * 16;
  const int half = tid & 1, row0 = tid >> 1;
  const u32x4 w0 = *reinterpret_cast<const u32x4*>(wq + (size_t)row0 * 4096 + col0 + half * 8);
  const u32x4 w1 = *reinterpret_cast<const u32x4*>(wq + (size_t)(row0 + 256) * 4096 + col0 + half * 8);
  const float ln = layernorm512(bf2f(x[tid]), red, tid);
  lnx[tid] = ln;
  __syncthreads();
  const float a0 = lnx[row0], a1 = lnx[row0 + 256];
  float acc[8];
#pragma unroll
  for (int k = 0; k < 4; ++k) {
    acc[2 * k] = a0 * bf_lo(w0[k]) + a1 * bf_lo(w1[k]);
    acc[2 * k + 1] = a0 * bf_hi(w0[k]) + a1 * bf_hi(w1[k]);
  }
#pragma unroll
  for (int k = 0; k < 8; ++k) {
#pragma unroll
    for (int o = 32; o >= 2; o >>= 1) acc[k] += __shfl_xor(acc[k], o);
  }
  if (lane < 2) {
#pragma unroll
    for (int k = 0; k < 8; ++k) part[wave][lane * 8 + k] = acc[k];
  }
  __syncthreads();
  if (tid < 16) {
    float s = 0.f;
#pragma unroll
    for (int w = 0; w < 8; ++w) s += part[w][tid];
    qout[col0 + tid] = f2bf(s);
  }
}

struct Tile { u32x4 k[8]; u32x4 v[8]; };

__device__ __forceinline__ void load_tile(Tile& t, const u16* kp, const u16* vp) {
#pragma unroll
  for (int s = 0; s < 2; ++s)
#pragma unroll
    for (int c = 0; c < 4; ++c)
      t.k[s * 4 + c] = __builtin_nontemporal_load(reinterpret_cast<const u32x4*>(kp + s * 16 * DH + c * 32));
#pragma unroll
  for (int i = 0; i < 8; ++i)
    t.v[i] = __builtin_nontemporal_load(reinterpret_cast<const u32x4*>(vp + ((i >> 2) * 16 + (i & 3)) * DH));
}

__device__ __forceinline__ void compute_tile(const Tile& t, const u32x4 (&qf)[4], float& m, float& l, f32x4 (&acc)[8]) {
  f32x4 s0 = {0.f, 0.f, 0.f, 0.f}, s1 = {0.f, 0.f, 0.f, 0.f};
#pragma unroll
  for (int c = 0; c < 4; ++c) {
    s0 = mfma16(t.k[c], qf[c], s0);
    s1 = mfma16(t.k[4 + c], qf[c], s1);
  }
  float sv[8];
#pragma unroll
  for (int r = 0; r < 4; ++r) {
    sv[r] = rbf(rbf(s0[r]) * SCALE);
    sv[4 + r] = rbf(rbf(s1[r]) * SCALE);
  }
  float tm = fmaxf(fmaxf(fmaxf(sv[0], sv[1]), fmaxf(sv[2], sv[3])), fmaxf(fmaxf(sv[4], sv[5]), fmaxf(sv[6], sv[7])));
  tm = xrow16_max(tm);
  if (__any(tm > m)) {
    const float mn = fmaxf(m, tm);
    const float alpha = __builtin_amdgcn_exp2f((m - mn) * L2E);
    m = mn;
    l *= alpha;
#pragma unroll
    for (int j = 0; j < 8; ++j) acc[j] *= alpha;
  }
  float p[8];
#pragma unroll
  for (int i = 0; i < 8; ++i) {
    p[i] = __builtin_amdgcn_exp2f((sv[i] - m) * L2E);
    l += p[i];
  }
  u32x4 pf;
#pragma unroll
  for (int u = 0; u < 4; ++u) pf[u] = pack_bf(p[2 * u], p[2 * u + 1]);
#pragma unroll
  for (int w = 0; w < 4; ++w) {
    u32x4 lo, hi;
#pragma unroll
    for (int u = 0; u < 4; ++u) {
      lo[u] = __builtin_amdgcn_perm(t.v[2 * u + 1][w], t.v[2 * u][w], 0x05040100u);
      hi[u] = __builtin_amdgcn_perm(t.v[2 * u + 1][w], t.v[2 * u][w], 0x07060302u);
    }
    acc[2 * w] = mfma16(lo, pf, acc[2 * w]);
    acc[2 * w + 1] = mfma16(hi, pf, acc[2 * w + 1]);
  }
}

__global__ __launch_bounds__(512, 2) void k_attn(const u16* __restrict__ K, const u16* __restrict__ V, const u16* __restrict__ q,
                                                 float* __restrict__ pm, float* __restrict__ pl, float* __restrict__ po) {
  __shared__ __attribute__((aligned(16))) float so[ATT_WAVES][4][DH];
  __shared__ float sm[ATT_WAVES][4], sl[ATT_WAVES][4];
  const int tid = threadIdx.x, lane = tid & 63, wave = __builtin_amdgcn_readfirstlane(tid >> 6);
  const int rho = lane & 15, g = lane >> 4;
  const int b = blockIdx.x, kvh = b & 7, sp = b >> 3;
  const size_t base = ((size_t)kvh * SK + (size_t)sp * KEYS_PER_WG + wave * 32) * DH;
  const u16* kp = K + base + rho * DH + g * 8;
  const u16* vp = V + base + (4 * g) * DH + rho * 8;
  constexpr int TSTRIDE = ATT_WAVES * 32 * DH;

  Tile A, B;
  load_tile(A, kp, vp);
  load_tile(B, kp + TSTRIDE, vp + TSTRIDE);

  u32x4 qf[4];
#pragma unroll
  for (int c = 0; c < 4; ++c) {
    qf[c] = u32x4{0u, 0u, 0u, 0u};
    if (rho < 4) qf[c] = *reinterpret_cast<const u32x4*>(q + (kvh * 4 + rho) * DH + c * 32 + g * 8);
  }
  float m = -INFINITY, l = 0.f;
  f32x4 acc[8];
#pragma unroll
  for (int j = 0; j < 8; ++j) acc[j] = f32x4{0.f, 0.f, 0.f, 0.f};

#pragma unroll
  for (int it = 0; it < TILES_PER_WAVE; it += 2) {
    compute_tile(A, qf, m, l, acc);
    if (it + 2 < TILES_PER_WAVE) load_tile(A, kp + (size_t)(it + 2) * TSTRIDE, vp + (size_t)(it + 2) * TSTRIDE);
    compute_tile(B, qf, m, l, acc);
    if (it + 3 < TILES_PER_WAVE) load_tile(B, kp + (size_t)(it + 3) * TSTRIDE, vp + (size_t)(it + 3) * TSTRIDE);
  }

  l = xrow16_sum(l);
  if (rho < 4) {
#pragma unroll
    for (int r = 0; r < 4; ++r) {
      f32x4 a = {acc[0][r], acc[1][r], acc[2][r], acc[3][r]};
      f32x4 c = {acc[4][r], acc[5][r], acc[6][r], acc[7][r]};
      *reinterpret_cast<f32x4*>(&so[wave][rho][32 * g + 8 * r]) = a;
      *reinterpret_cast<f32x4*>(&so[wave][rho][32 * g + 8 * r + 4]) = c;
    }
    if (g == 0) { sm[wave][rho] = m; sl[wave][rho] = l; }
  }
  __syncthreads();
  {
    const int h = tid >> 7, d = tid & 127;
    float M = sm[0][h];
#pragma unroll
    for (int w = 1; w < ATT_WAVES; ++w) M = fmaxf(M, sm[w][h]);
    float L = 0.f, O = 0.f;
#pragma unroll
    for (int w = 0; w < ATT_WAVES; ++w) {
      const float e = __builtin_amdgcn_exp2f((sm[w][h] - M) * L2E);
      L += e * sl[w][h];
      O += e * so[w][h][d];
    }
    const int slot = (kvh * 4 + h) * NSPLIT + sp;
    po[(size_t)slot * DH + d] = O;
    if (d == 0) { pm[slot] = M; pl[slot] = L; }
  }
}

__global__ __launch_bounds__(512) void k_oproj(const float* __restrict__ pm, const float* __restrict__ pl, const float* __restrict__ po,
                                               const u16* __restrict__ wo, float* __restrict__ y) {
  __shared__ float so[4][DH];
  __shared__ float oh[DH];
  __shared__ __attribute__((aligned(16))) float red[8][D_MODEL];
  const int tid = threadIdx.x, lane = tid & 63, wave = tid >> 6;
  const int h = blockIdx.x;
  u32x4 w[16];
#pragma unroll
  for (int i = 0; i < 16; ++i)
    w[i] = *reinterpret_cast<const u32x4*>(wo + (size_t)(h * DH + wave * 16 + i) * D_MODEL + lane * 8);
  {
    const int d = tid & 127, qd = tid >> 7;
    float M = -INFINITY;
#pragma unroll
    for (int s = 0; s < NSPLIT; ++s) M = fmaxf(M, pm[h * NSPLIT + s]);
    float L = 0.f;
#pragma unroll
    for (int s = 0; s < NSPLIT; ++s) L += __builtin_amdgcn_exp2f((pm[h * NSPLIT + s] - M) * L2E) * pl[h * NSPLIT + s];
    float O = 0.f;
#pragma unroll
    for (int s = qd * 8; s < qd * 8 + 8; ++s)
      O += __builtin_amdgcn_exp2f((pm[h * NSPLIT + s] - M) * L2E) * po[(size_t)(h * NSPLIT + s) * DH + d];
    so[qd][d] = O / L;
  }
  __syncthreads();
  if (tid < DH) oh[tid] = rbf((so[0][tid] + so[1][tid]) + (so[2][tid] + so[3][tid]));
  __syncthreads();
  float acc[8];
#pragma unroll
  for (int k = 0; k < 8; ++k) acc[k] = 0.f;
#pragma unroll
  for (int i = 0; i < 16; ++i) {
    const float a = oh[wave * 16 + i];
#pragma unroll
    for (int k = 0; k < 4; ++k) {
      acc[2 * k] += a * bf_lo(w[i][k]);
      acc[2 * k + 1] += a * bf_hi(w[i][k]);
    }
  }
  *reinterpret_cast<f32x4*>(&red[wave][lane * 8]) = f32x4{acc[0], acc[1], acc[2], acc[3]};
  *reinterpret_cast<f32x4*>(&red[wave][lane * 8 + 4]) = f32x4{acc[4], acc[5], acc[6], acc[7]};
  __syncthreads();
  float s = 0.f;
#pragma unroll
  for (int w8 = 0; w8 < 8; ++w8) s += red[w8][tid];
  y[h * D_MODEL + tid] = s;
}

__global__ __launch_bounds__(512) void k_mlp(const u16* __restrict__ x, const float* __restrict__ y, const u16* __restrict__ w1,
                                             const u16* __restrict__ b1, const u16* __restrict__ w2, float* __restrict__ z,
                                             float* __restrict__ hout) {
  __shared__ float red[16];
  __shared__ float lnx[D_MODEL];
  __shared__ float part[8][64];
  __shared__ float hid[64];
  __shared__ __attribute__((aligned(16))) float red2[8][D_MODEL];
  const int tid = threadIdx.x, lane = tid & 63, wave = tid >> 6;
  const int c = blockIdx.x;
  const int cg = tid & 7, r0 = tid >> 3;
  u32x4 wa[8], wb[8];
#pragma unroll
  for (int i = 0; i < 8; ++i)
    wa[i] = *reinterpret_cast<const u32x4*>(w1 + (size_t)(r0 + 64 * i) * FF + c * 64 + cg * 8);
#pragma unroll
  for (int i = 0; i < 8; ++i)
    wb[i] = *reinterpret_cast<const u32x4*>(w2 + (size_t)(c * 64 + wave * 8 + i) * D_MODEL + lane * 8);
  float s = 0.f;
#pragma unroll
  for (int hh = 0; hh < HQ; ++hh) s += y[hh * D_MODEL + tid];
  const float hv = rbf(bf2f(x[tid]) + rbf(s));
  if (c == 0) hout[tid] = hv;
  const float ln = layernorm512(hv, red, tid);
  lnx[tid] = ln;
  __syncthreads();
  float acc[8];
#pragma unroll
  for (int k = 0; k < 8; ++k) acc[k] = 0.f;
#pragma unroll
  for (int i = 0; i < 8; ++i) {
    const float a = lnx[r0 + 64 * i];
#pragma unroll
    for (int k = 0; k < 4; ++k) {
      acc[2 * k] += a * bf_lo(wa[i][k]);
      acc[2 * k + 1] += a * bf_hi(wa[i][k]);
    }
  }
#pragma unroll
  for (int k = 0; k < 8; ++k) {
    acc[k] += __shfl_xor(acc[k], 8);
    acc[k] += __shfl_xor(acc[k], 16);
    acc[k] += __shfl_xor(acc[k], 32);
  }
  if (lane < 8) {
#pragma unroll
    for (int k = 0; k < 8; ++k) part[wave][lane * 8 + k] = acc[k];
  }
  __syncthreads();
  if (tid < 64) {
    float t = 0.f;
#pragma unroll
    for (int w = 0; w < 8; ++w) t += part[w][tid];
    const float h1 = rbf(rbf(t) + bf2f(b1[c * 64 + tid]));
    const float r = h1 > 0.f ? h1 : 0.f;
    hid[tid] = rbf(r * r);
  }
  __syncthreads();
#pragma unroll
  for (int k = 0; k < 8; ++k) acc[k] = 0.f;
#pragma unroll
  for (int i = 0; i < 8; ++i) {
    const float a = hid[wave * 8 + i];
#pragma unroll
    for (int k = 0; k < 4; ++k) {
      acc[2 * k] += a * bf_lo(wb[i][k]);
      acc[2 * k + 1] += a * bf_hi(wb[i][k]);
    }
  }
  *reinterpret_cast<f32x4*>(&red2[wave][lane * 8]) = f32x4{acc[0], acc[1], acc[2], acc[3]};
  *reinterpret_cast<f32x4*>(&red2[wave][lane * 8 + 4]) = f32x4{acc[4], acc[5], acc[6], acc[7]};
  __syncthreads();
  float t = 0.f;
#pragma unroll
  for (int w8 = 0; w8 < 8; ++w8) t += red2[w8][tid];
  z[c * D_MODEL + tid] = t;
}

__global__ __launch_bounds__(512) void k_final(const float* __restrict__ z, const float* __restrict__ hin, const u16* __restrict__ b2,
                                               u16* __restrict__ out) {
  const int tid = threadIdx.x;
  float s = 0.f;
#pragma unroll
  for (int c = 0; c < NC; ++c) s += z[c * D_MODEL + tid];
  const float o = rbf(rbf(s) + bf2f(b2[tid]));
  out[tid] = f2bf(hin[tid] + o);
}

extern "C" void kernel_launch(void* const* d_in, const int* in_sizes, int n_in, void* d_out, int out_size, void* d_ws, size_t ws_size,
                              hipStream_t stream) {
  const u16* x = (const u16*)d_in[0];
  const u16* kc = (const u16*)d_in[1];
  const u16* vc = (const u16*)d_in[2];
  const u16* wq = (const u16*)d_in[3];
  const u16* wo = (const u16*)d_in[4];
  const u16* w1 = (const u16*)d_in[6];
  const u16* b1 = (const u16*)d_in[7];
  const u16* w2 = (const u16*)d_in[8];
  const u16* b2 = (const u16*)d_in[9];
  char* ws = (char*)d_ws;
  u16* q = (u16*)(ws + WS_Q);
  float* pm = (float*)(ws + WS_PM);
  float* pl = (float*)(ws + WS_PL);
  float* po = (float*)(ws + WS_PO);
  float* y = (float*)(ws + WS_Y);
  float* z = (float*)(ws + WS_Z);
  float* hb = (float*)(ws + WS_H);
  k_qproj<<<HKV * NSPLIT, 512, 0, stream>>>(x, wq, q);
  k_attn<<<HKV * NSPLIT, 512, 0, stream>>>(kc, vc, q, pm, pl, po);
  k_oproj<<<HQ, 512, 0, stream>>>(pm, pl, po, wo, y);
  k_mlp<<<NC, 512, 0, stream>>>(x, y, w1, b1, w2, z, hb);
  k_final<<<1, 512, 0, stream>>>(z, hb, b2, (u16*)d_out);
}
```

```cpp
#include <hip/hip_runtime.h>
#include <hip/hip_bf16.h>
#include <stdint.h>

typedef unsigned short u16;
typedef unsigned int u32;
typedef unsigned long long u64;
typedef u32 u32x4 __attribute__((ext_vector_type(4)));
typedef float f32x4 __attribute__((ext_vector_type(4)));
typedef __bf16 bf16x8 __attribute__((ext_vector_type(8)));

constexpr int D_MODEL = 512, HQ = 32, HKV = 8, SK = 65536, DH = 128, FF = 1792;
constexpr int NSPLIT = 32;
constexpr int NWG = HKV * NSPLIT;
constexpr int KEYS_PER_WG = SK / NSPLIT;
constexpr int ATT_WAVES = 8;
constexpr int TILES_PER_WAVE = KEYS_PER_WG / (32 * ATT_WAVES);
constexpr int NB = 32, NC = 28, ND = 8, NTAIL = NB + NC + ND;
constexpr float SCALE = 0.08838834764831845f;
constexpr float L2E = 1.4426950408889634f;
constexpr u32 SPIN_CAP = 1u << 18;
constexpr int AUX_SC1 = 16;

constexpr u32 WS_E = 0;
constexpr u32 WS_QG = 256;
constexpr u32 WS_PO = WS_QG + 4096 * 8;
constexpr u32 WS_PML = WS_PO + HQ * NSPLIT * 64 * 16;
constexpr u32 WS_Y = WS_PML + HQ * NSPLIT * 16;
constexpr u32 WS_Z = WS_Y + HQ * 256 * 16;
constexpr u32 WS_END = WS_Z + NC * 256 * 16;

__device__ __forceinline__ float rbf(float x) { return (float)(__bf16)x; }
__device__ __forceinline__ float bf_lo(u32 w) { return __builtin_bit_cast(float, w << 16); }
__device__ __forceinline__ float bf_hi(u32 w) { return __builtin_bit_cast(float, w & 0xffff0000u); }
__device__ __forceinline__ float bf2f(u16 v) { return __builtin_bit_cast(float, (u32)v << 16); }
__device__ __forceinline__ u16 f2bf(float x) { return __builtin_bit_cast(u16, (__bf16)x); }
__device__ __forceinline__ u32 pack_bf(float lo, float hi) { return (u32)f2bf(lo) | ((u32)f2bf(hi) << 16); }
__device__ __forceinline__ u32 f2u(float x) { return __builtin_bit_cast(u32, x); }
__device__ __forceinline__ float u2f(u32 x) { return __builtin_bit_cast(float, x); }

__device__ __forceinline__ f32x4 mfma16(u32x4 a, u32x4 b, f32x4 c) {
  return __builtin_amdgcn_mfma_f32_16x16x32_bf16(__builtin_bit_cast(bf16x8, a), __builtin_bit_cast(bf16x8, b), c, 0, 0, 0);
}
__device__ __forceinline__ float xrow16_max(float x) {
  auto s = __builtin_amdgcn_permlane16_swap(f2u(x), f2u(x), false, false);
  x = fmaxf(u2f(s[0]), u2f(s[1]));
  auto t = __builtin_amdgcn_permlane32_swap(f2u(x), f2u(x), false, false);
  return fmaxf(u2f(t[0]), u2f(t[1]));
}
__device__ __forceinline__ float xrow16_sum(float x) {
  auto s = __builtin_amdgcn_permlane16_swap(f2u(x), f2u(x), false, false);
  x = u2f(s[0]) + u2f(s[1]);
  auto t = __builtin_amdgcn_permlane32_swap(f2u(x), f2u(x), false, false);
  return u2f(t[0]) + u2f(t[1]);
}
__device__ __forceinline__ float wave_sum(float v) {
#pragma unroll
  for (int o = 32; o >= 1; o >>= 1) v += __shfl_xor(v, o);
  return v;
}
__device__ __forceinline__ void wg_barrier() {
  asm volatile("s_waitcnt lgkmcnt(0)" ::: "memory");
  __builtin_amdgcn_s_barrier();
  asm volatile("" ::: "memory");
}
__device__ __forceinline__ u32x4 ld_sc1(__amdgpu_buffer_rsrc_t rs, u32 off) {
  return __builtin_amdgcn_raw_buffer_load_b128(rs, (int)off, 0, AUX_SC1);
}
__device__ __forceinline__ void st_sc1(__amdgpu_buffer_rsrc_t rs, u32 off, u32x4 v) {
  __builtin_amdgcn_raw_buffer_store_b128(v, rs, (int)off, 0, AUX_SC1);
}

__device__ __forceinline__ float layernorm512(float xv, float* red, int tid) {
  float s = wave_sum(xv);
  if ((tid & 63) == 0) red[tid >> 6] = s;
  wg_barrier();
  float tot = 0.f;
#pragma unroll
  for (int w = 0; w < 8; ++w) tot += red[w];
  const float mean = tot * (1.0f / 512.0f);
  const float dv = xv - mean;
  float s2 = wave_sum(dv * dv);
  if ((tid & 63) == 0) red[8 + (tid >> 6)] = s2;
  wg_barrier();
  float tot2 = 0.f;
#pragma unroll
  for (int w = 0; w < 8; ++w) tot2 += red[8 + w];
  const float var = rbf(tot2 * (1.0f / 512.0f));
  const float mb = rbf(mean);
  const float den = rbf(sqrtf(rbf(var + 1e-5f)));
  return rbf(rbf(xv - mb) / den);
}

struct Tile { u32x4 k[8]; u32x4 v[8]; };

__device__ __forceinline__ void load_tile(Tile& t, const u16* kp, const u16* vp) {
#pragma unroll
  for (int s = 0; s < 2; ++s)
#pragma unroll
    for (int c = 0; c < 4; ++c)
      t.k[s * 4 + c] = __builtin_nontemporal_load(reinterpret_cast<const u32x4*>(kp + s * 16 * DH + c * 32));
  __builtin_amdgcn_sched_barrier(0);
#pragma unroll
  for (int i = 0; i < 8; ++i)
    t.v[i] = __builtin_nontemporal_load(reinterpret_cast<const u32x4*>(vp + ((i >> 2) * 16 + (i & 3)) * DH));
  __builtin_amdgcn_sched_barrier(0);
}

__device__ __forceinline__ void compute_tile(const Tile& t, const u32x4 (&qf)[4], float& m, float& l, f32x4 (&acc)[8]) {
  f32x4 s0 = {0.f, 0.f, 0.f, 0.f}, s1 = {0.f, 0.f, 0.f, 0.f};
#pragma unroll
  for (int c = 0; c < 4; ++c) {
    s0 = mfma16(t.k[c], qf[c], s0);
    s1 = mfma16(t.k[4 + c], qf[c], s1);
  }
  float sv[8];
#pragma unroll
  for (int r = 0; r < 4; ++r) {
    sv[r] = rbf(rbf(s0[r]) * SCALE);
    sv[4 + r] = rbf(rbf(s1[r]) * SCALE);
  }
  float tm = fmaxf(fmaxf(fmaxf(sv[0], sv[1]), fmaxf(sv[2], sv[3])), fmaxf(fmaxf(sv[4], sv[5]), fmaxf(sv[6], sv[7])));
  tm = xrow16_max(tm);
  if (__any(tm > m)) {
    const float mn = fmaxf(m, tm);
    const float alpha = __builtin_amdgcn_exp2f((m - mn) * L2E);
    m = mn;
    l *= alpha;
#pragma unroll
    for (int j = 0; j < 8; ++j) acc[j] *= alpha;
  }
  float p[8];
#pragma unroll
  for (int i = 0; i < 8; ++i) {
    p[i] = __builtin_amdgcn_exp2f((sv[i] - m) * L2E);
    l += p[i];
  }
  u32x4 pf;
#pragma unroll
  for (int u = 0; u < 4; ++u) pf[u] = pack_bf(p[2 * u], p[2 * u + 1]);
#pragma unroll
  for (int w = 0; w < 4; ++w) {
    u32x4 lo, hi;
#pragma unroll
    for (int u = 0; u < 4; ++u) {
      lo[u] = __builtin_amdgcn_perm(t.v[2 * u + 1][w], t.v[2 * u][w], 0x05040100u);
      hi[u] = __builtin_amdgcn_perm(t.v[2 * u + 1][w], t.v[2 * u][w], 0x07060302u);
    }
    acc[2 * w] = mfma16(lo, pf, acc[2 * w]);
    acc[2 * w + 1] = mfma16(hi, pf, acc[2 * w + 1]);
  }
}

__device__ __forceinline__ void gemv_rows(float (&acc)[8], const u32x4* w, const float* a, int n) {
  for (int i = 0; i < n; ++i) {
    const float s = a[i];
#pragma unroll
    for (int k = 0; k < 4; ++k) {
      acc[2 * k] += s * bf_lo(w[i][k]);
      acc[2 * k + 1] += s * bf_hi(w[i][k]);
    }
  }
}

struct Smem {
  float big[8 * 512];
  float big2[8 * 512];
  float lnx[512];
  float sm[8][4], sl[8][4];
  float red[16];
  float part[8][64];
  float vec[128];
  u32 qs[256];
  u64 epoch;
};

__global__ __launch_bounds__(512, 2) void k_fused(const u16* __restrict__ x, const u16* __restrict__ K, const u16* __restrict__ V,
                                                  const u16* __restrict__ wq, const u16* __restrict__ wo, const u16* __restrict__ w1,
                                                  const u16* __restrict__ b1, const u16* __restrict__ w2, const u16* __restrict__ b2,
                                                  u16* __restrict__ out, unsigned char* ws) {
  __shared__ __attribute__((aligned(16))) Smem sh;
  const int tid = threadIdx.x, lane = tid & 63, wave = __builtin_amdgcn_readfirstlane(tid >> 6);
  const int rho = lane & 15, g = lane >> 4;
  const int b = blockIdx.x, kvh = b & 7, sp = b >> 3;
  const __amdgpu_buffer_rsrc_t rs = __builtin_amdgcn_make_buffer_rsrc(ws, 0, (int)WS_END, 0x00020000);
  u64* eptr = reinterpret_cast<u64*>(ws + WS_E);

  const size_t base = ((size_t)kvh * SK + (size_t)sp * KEYS_PER_WG + wave * 32) * DH;
  const u16* kp = K + base + rho * DH + g * 8;
  const u16* vp = V + base + (4 * g) * DH + rho * 8;
  constexpr int TSTRIDE = ATT_WAVES * 32 * DH;

  if (wave == ATT_WAVES - 1) {
    const u64 epoch = __hip_atomic_load(eptr, __ATOMIC_RELAXED, __HIP_MEMORY_SCOPE_AGENT) + 1ull;
    const u32x4 zero4 = {0u, 0u, 0u, 0u};
    const int col0 = kvh * 512 + sp * 16;
    const u32x4 xv = *reinterpret_cast<const u32x4*>(x + lane * 8);
    u32x4 wv[16];
#pragma unroll
    for (int i = 0; i < 16; ++i)
      wv[i] = *reinterpret_cast<const u32x4*>(wq + (size_t)((lane >> 1) + 32 * i) * 4096 + col0 + (lane & 1) * 8);
#pragma unroll
    for (int h = 0; h < 4; ++h) st_sc1(rs, WS_PO + (u32)(((kvh * 4 + h) * NSPLIT + sp) * 64 + lane) * 16, zero4);
    if (lane < 4) st_sc1(rs, WS_PML + (u32)((kvh * 4 + lane) * NSPLIT + sp) * 16, zero4);
    if (b < NB) {
      const int hb = (b & 7) * 4 + (b >> 3);
#pragma unroll
      for (int i = 0; i < 4; ++i) st_sc1(rs, WS_Y + (u32)(hb * 256 + i * 64 + lane) * 16, zero4);
    } else if (b < NB + NC) {
#pragma unroll
      for (int i = 0; i < 4; ++i) st_sc1(rs, WS_Z + (u32)((b - NB) * 256 + i * 64 + lane) * 16, zero4);
    }
    float xf[8];
#pragma unroll
    for (int k = 0; k < 4; ++k) { xf[2 * k] = bf_lo(xv[k]); xf[2 * k + 1] = bf_hi(xv[k]); }
    float s = 0.f;
#pragma unroll
    for (int k = 0; k < 8; ++k) s += xf[k];
    const float mean = wave_sum(s) * (1.0f / 512.0f);
    float s2 = 0.f;
#pragma unroll
    for (int k = 0; k < 8; ++k) s2 += (xf[k] - mean) * (xf[k] - mean);
    const float var = rbf(wave_sum(s2) * (1.0f / 512.0f));
    const float mb = rbf(mean);
    const float den = rbf(sqrtf(rbf(var + 1e-5f)));
#pragma unroll
    for (int k = 0; k < 8; ++k) sh.lnx[lane * 8 + k] = rbf(rbf(xf[k] - mb) / den);
    asm volatile("s_waitcnt lgkmcnt(0)" ::: "memory");
    float acc[8];
#pragma unroll
    for (int k = 0; k < 8; ++k) acc[k] = 0.f;
#pragma unroll
    for (int i = 0; i < 16; ++i) {
      const float a = sh.lnx[(lane >> 1) + 32 * i];
#pragma unroll
      for (int k = 0; k < 4; ++k) {
        acc[2 * k] += a * bf_lo(wv[i][k]);
        acc[2 * k + 1] += a * bf_hi(wv[i][k]);
      }
    }
#pragma unroll
    for (int k = 0; k < 8; ++k) {
#pragma unroll
      for (int o = 32; o >= 2; o >>= 1) acc[k] += __shfl_xor(acc[k], o);
    }
    const u32 tlo = (u32)(epoch << 16), thi = (u32)(epoch >> 16);
    asm volatile("s_waitcnt vmcnt(0)" ::: "memory");
    if (lane < 2) {
#pragma unroll
      for (int k = 0; k < 4; ++k) {
        const u32x4 gv = {tlo | (u32)f2bf(acc[2 * k]), thi, tlo | (u32)f2bf(acc[2 * k + 1]), thi};
        st_sc1(rs, WS_QG + (u32)(col0 + lane * 8 + 2 * k) * 8, gv);
      }
    }
    const bool tailwg = b < NTAIL;
    const u32 toff0 = WS_QG + (u32)((lane & 7) * 512 + (lane >> 3) * 16) * 8;
    const int b2i = 64 + (lane & 3);
    const u32 toff1 = WS_QG + (u32)((b2i & 7) * 512 + (b2i >> 3) * 16) * 8;
    u32x4 qv[4];
    for (u32 spin = 0;; ++spin) {
      asm volatile("" ::: "memory");
      bool ok = true;
#pragma unroll
      for (int j = 0; j < 4; ++j) qv[j] = ld_sc1(rs, WS_QG + (u32)(kvh * 512) * 8 + (u32)(lane + 64 * j) * 16);
      if (tailwg) {
        const u32x4 t0 = ld_sc1(rs, toff0), t1 = ld_sc1(rs, toff1);
        ok = ((t0[0] & 0xffff0000u) == tlo) && (t0[1] == thi) && ((t1[0] & 0xffff0000u) == tlo) && (t1[1] == thi);
      }
#pragma unroll
      for (int j = 0; j < 4; ++j)
        ok = ok && ((qv[j][0] & 0xffff0000u) == tlo) && (qv[j][1] == thi) && ((qv[j][2] & 0xffff0000u) == tlo) && (qv[j][3] == thi);
      if (__all(ok) || spin > SPIN_CAP) break;
      __builtin_amdgcn_s_sleep(1);
    }
#pragma unroll
    for (int j = 0; j < 4; ++j) sh.qs[lane + 64 * j] = (qv[j][0] & 0xffffu) | (qv[j][2] << 16);
    if (lane == 0) sh.epoch = epoch;
  }

  Tile A, B;
  load_tile(A, kp, vp);
  load_tile(B, kp + TSTRIDE, vp + TSTRIDE);
  wg_barrier();
  const u64 epoch = sh.epoch;
  const u32 tag = (u32)epoch * 2u + 1u;

  u32x4 qf[4];
#pragma unroll
  for (int c = 0; c < 4; ++c) {
    qf[c] = u32x4{0u, 0u, 0u, 0u};
    if (rho < 4) qf[c] = *reinterpret_cast<const u32x4*>(&sh.qs[rho * 64 + c * 16 + g * 4]);
  }
  float m = -INFINITY, l = 0.f;
  f32x4 acc[8];
#pragma unroll
  for (int j = 0; j < 8; ++j) acc[j] = f32x4{0.f, 0.f, 0.f, 0.f};

#pragma unroll
  for (int it = 0; it < TILES_PER_WAVE - 2; it += 2) {
    compute_tile(A, qf, m, l, acc);
    load_tile(A, kp + (size_t)(it + 2) * TSTRIDE, vp + (size_t)(it + 2) * TSTRIDE);
    compute_tile(B, qf, m, l, acc);
    load_tile(B, kp + (size_t)(it + 3) * TSTRIDE, vp + (size_t)(it + 3) * TSTRIDE);
  }
  compute_tile(A, qf, m, l, acc);

  u32x4 W[16];
  const int hb = (b & 7) * 4 + (b >> 3);
  const int cs = b - NB;
  if (b < NB) {
#pragma unroll
    for (int i = 0; i < 16; ++i)
      W[i] = *reinterpret_cast<const u32x4*>(wo + (size_t)(hb * DH + wave * 16 + i) * D_MODEL + lane * 8);
    compute_tile(B, qf, m, l, acc);
  } else if (b < NB + NC) {
#pragma unroll
    for (int i = 0; i < 8; ++i)
      W[i] = *reinterpret_cast<const u32x4*>(w1 + (size_t)((tid >> 3) + 64 * i) * FF + cs * 64 + (tid & 7) * 8);
#pragma unroll
    for (int i = 0; i < 8; ++i)
      W[8 + i] = *reinterpret_cast<const u32x4*>(w2 + (size_t)(cs * 64 + wave * 8 + i) * D_MODEL + lane * 8);
    compute_tile(B, qf, m, l, acc);
  } else {
    compute_tile(B, qf, m, l, acc);
  }

  l = xrow16_sum(l);
  if (rho < 4) {
#pragma unroll
    for (int r = 0; r < 4; ++r) {
      f32x4 a = {acc[0][r], acc[1][r], acc[2][r], acc[3][r]};
      f32x4 c = {acc[4][r], acc[5][r], acc[6][r], acc[7][r]};
      float* dst = &sh.big[(wave * 4 + rho) * DH + 32 * g + 8 * r];
      *reinterpret_cast<f32x4*>(dst) = a;
      *reinterpret_cast<f32x4*>(dst + 4) = c;
    }
    if (g == 0) { sh.sm[wave][rho] = m; sh.sl[wave][rho] = l; }
  }
  wg_barrier();
  if (tid < 256) {
    const int h = tid >> 6, dp = tid & 63;
    float M = sh.sm[0][h];
#pragma unroll
    for (int w = 1; w < ATT_WAVES; ++w) M = fmaxf(M, sh.sm[w][h]);
    float L = 0.f, O0 = 0.f, O1 = 0.f;
#pragma unroll
    for (int w = 0; w < ATT_WAVES; ++w) {
      const float e = __builtin_amdgcn_exp2f((sh.sm[w][h] - M) * L2E);
      L += e * sh.sl[w][h];
      O0 += e * sh.big[(w * 4 + h) * DH + 2 * dp];
      O1 += e * sh.big[(w * 4 + h) * DH + 2 * dp + 1];
    }
    const u32 slot = (u32)((kvh * 4 + h) * NSPLIT + sp);
    st_sc1(rs, WS_PO + (slot * 64 + dp) * 16, u32x4{f2u(O0), tag, f2u(O1), tag});
    if (dp == 0) st_sc1(rs, WS_PML + slot * 16, u32x4{f2u(M), tag, f2u(L), tag});
  }
  if (b >= NTAIL) return;

  if (b < NB) {
    u32x4 ml, o4[4];
    for (u32 spin = 0;; ++spin) {
      asm volatile("" ::: "memory");
      ml = ld_sc1(rs, WS_PML + (u32)(hb * NSPLIT + (lane & 31)) * 16);
#pragma unroll
      for (int j = 0; j < 4; ++j) o4[j] = ld_sc1(rs, WS_PO + (u32)((hb * NSPLIT + wave * 4 + j) * 64 + lane) * 16);
      bool ok = ml[1] == tag && ml[3] == tag;
#pragma unroll
      for (int j = 0; j < 4; ++j) ok = ok && o4[j][1] == tag && o4[j][3] == tag;
      if (__all(ok) || spin > SPIN_CAP) break;
      __builtin_amdgcn_s_sleep(1);
    }
    const float ms = u2f(ml[0]);
    float M = ms;
#pragma unroll
    for (int o = 16; o >= 1; o >>= 1) M = fmaxf(M, __shfl_xor(M, o));
    const float wgt = __builtin_amdgcn_exp2f((ms - M) * L2E);
    float L = wgt * u2f(ml[2]);
#pragma unroll
    for (int o = 16; o >= 1; o >>= 1) L += __shfl_xor(L, o);
    float o0 = 0.f, o1 = 0.f;
#pragma unroll
    for (int j = 0; j < 4; ++j) {
      const float wj = __shfl(wgt, wave * 4 + j);
      o0 += wj * u2f(o4[j][0]);
      o1 += wj * u2f(o4[j][2]);
    }
    sh.big2[wave * DH + 2 * lane] = o0;
    sh.big2[wave * DH + 2 * lane + 1] = o1;
    wg_barrier();
    if (tid < DH) {
      float s = 0.f;
#pragma unroll
      for (int w = 0; w < 8; ++w) s += sh.big2[w * DH + tid];
      sh.vec[tid] = rbf(s / L);
    }
    wg_barrier();
    float a8[8];
#pragma unroll
    for (int k = 0; k < 8; ++k) a8[k] = 0.f;
#pragma unroll
    for (int i = 0; i < 16; ++i) {
      const float a = sh.vec[wave * 16 + i];
#pragma unroll
      for (int k = 0; k < 4; ++k) {
        a8[2 * k] += a * bf_lo(W[i][k]);
        a8[2 * k + 1] += a * bf_hi(W[i][k]);
      }
    }
    *reinterpret_cast<f32x4*>(&sh.big[wave * 512 + lane * 8]) = f32x4{a8[0], a8[1], a8[2], a8[3]};
    *reinterpret_cast<f32x4*>(&sh.big[wave * 512 + lane * 8 + 4]) = f32x4{a8[4], a8[5], a8[6], a8[7]};
    wg_barrier();
    if (tid < 256) {
      float y0 = 0.f, y1 = 0.f;
#pragma unroll
      for (int w = 0; w < 8; ++w) { y0 += sh.big[w * 512 + 2 * tid]; y1 += sh.big[w * 512 + 2 * tid + 1]; }
      st_sc1(rs, WS_Y + (u32)(hb * 256 + tid) * 16, u32x4{f2u(y0), tag, f2u(y1), tag});
    }
  } else if (b < NB + NC) {
    const int cp = tid & 255, hh = tid >> 8;
    const float xres = bf2f(x[tid]);
    const float bias1 = bf2f(b1[cs * 64 + (tid & 63)]);
    u32x4 yv[16];
    for (u32 spin = 0;; ++spin) {
      asm volatile("" ::: "memory");
#pragma unroll
      for (int j = 0; j < 16; ++j) yv[j] = ld_sc1(rs, WS_Y + (u32)((hh * 16 + j) * 256 + cp) * 16);
      bool ok = true;
#pragma unroll
      for (int j = 0; j < 16; ++j) ok = ok && yv[j][1] == tag && yv[j][3] == tag;
      if (__all(ok) || spin > SPIN_CAP) break;
      __builtin_amdgcn_s_sleep(1);
    }
    float s0 = 0.f, s1 = 0.f;
#pragma unroll
    for (int j = 0; j < 16; ++j) { s0 += u2f(yv[j][0]); s1 += u2f(yv[j][2]); }
    sh.big2[hh * 512 + 2 * cp] = s0;
    sh.big2[hh * 512 + 2 * cp + 1] = s1;
    wg_barrier();
    const float hv = rbf(xres + rbf(sh.big2[tid] + sh.big2[512 + tid]));
    const float ln = layernorm512(hv, sh.red, tid);
    sh.lnx[tid] = ln;
    wg_barrier();
    float a8[8];
#pragma unroll
    for (int k = 0; k < 8; ++k) a8[k] = 0.f;
#pragma unroll
    for (int i = 0; i < 8; ++i) {
      const float a = sh.lnx[(tid >> 3) + 64 * i];
#pragma unroll
      for (int k = 0; k < 4; ++k) {
        a8[2 * k] += a * bf_lo(W[i][k]);
        a8[2 * k + 1] += a * bf_hi(W[i][k]);
      }
    }
#pragma unroll
    for (int k = 0; k < 8; ++k) {
      a8[k] += __shfl_xor(a8[k], 8);
      a8[k] += __shfl_xor(a8[k], 16);
      a8[k] += __shfl_xor(a8[k], 32);
    }
    if (lane < 8) {
#pragma unroll
      for (int k = 0; k < 8; ++k) sh.part[wave][lane * 8 + k] = a8[k];
    }
    wg_barrier();
    if (tid < 64) {
      float t = 0.f;
#pragma unroll
      for (int w = 0; w < 8; ++w) t += sh.part[w][tid];
      const float h1 = rbf(rbf(t) + bias1);
      const float r = h1 > 0.f ? h1 : 0.f;
      sh.vec[tid] = rbf(r * r);
    }
    wg_barrier();
#pragma unroll
    for (int k = 0; k < 8; ++k) a8[k] = 0.f;
#pragma unroll
    for (int i = 0; i < 8; ++i) {
      const float a = sh.vec[wave * 8 + i];
#pragma unroll
      for (int k = 0; k < 4; ++k) {
        a8[2 * k] += a * bf_lo(W[8 + i][k]);
        a8[2 * k + 1] += a * bf_hi(W[8 + i][k]);
      }
    }
    *reinterpret_cast<f32x4*>(&sh.big[wave * 512 + lane * 8]) = f32x4{a8[0], a8[1], a8[2], a8[3]};
    *reinterpret_cast<f32x4*>(&sh.big[wave * 512 + lane * 8 + 4]) = f32x4{a8[4], a8[5], a8[6], a8[7]};
    wg_barrier();
    if (tid < 256) {
      float z0 = 0.f, z1 = 0.f;
#pragma unroll
      for (int w = 0; w < 8; ++w) { z0 += sh.big[w * 512 + 2 * tid]; z1 += sh.big[w * 512 + 2 * tid + 1]; }
      st_sc1(rs, WS_Z + (u32)(cs * 256 + tid) * 16, u32x4{f2u(z0), tag, f2u(z1), tag});
    }
  } else {
    const int dj = b - NB - NC;
    const int pp = tid & 31, qq = tid >> 5;
    const int col = dj * 64 + (tid & 63);
    const float xres = bf2f(x[col]);
    const float bias2 = bf2f(b2[col]);
    u32x4 y0, y1;
    for (u32 spin = 0;; ++spin) {
      asm volatile("" ::: "memory");
      y0 = ld_sc1(rs, WS_Y + (u32)((2 * qq) * 256 + dj * 32 + pp) * 16);
      y1 = ld_sc1(rs, WS_Y + (u32)((2 * qq + 1) * 256 + dj * 32 + pp) * 16);
      const bool ok = y0[1] == tag && y0[3] == tag && y1[1] == tag && y1[3] == tag;
      if (__all(ok) || spin > SPIN_CAP) break;
      __builtin_amdgcn_s_sleep(1);
    }
    sh.big2[(2 * qq) * 64 + 2 * pp] = u2f(y0[0]);
    sh.big2[(2 * qq) * 64 + 2 * pp + 1] = u2f(y0[2]);
    sh.big2[(2 * qq + 1) * 64 + 2 * pp] = u2f(y1[0]);
    sh.big2[(2 * qq + 1) * 64 + 2 * pp + 1] = u2f(y1[2]);
    wg_barrier();
    float hv = 0.f;
    if (tid < 64) {
      float s0 = 0.f, s1 = 0.f;
#pragma unroll
      for (int j = 0; j < 16; ++j) { s0 += sh.big2[j * 64 + tid]; s1 += sh.big2[(16 + j) * 64 + tid]; }
      hv = rbf(xres + rbf(s0 + s1));
    }
    const int c1 = qq + 16 < NC ? qq + 16 : qq;
    u32x4 z0, z1;
    for (u32 spin = 0;; ++spin) {
      asm volatile("" ::: "memory");
      z0 = ld_sc1(rs, WS_Z + (u32)(qq * 256 + dj * 32 + pp) * 16);
      z1 = ld_sc1(rs, WS_Z + (u32)(c1 * 256 + dj * 32 + pp) * 16);
      const bool ok = z0[1] == tag && z0[3] == tag && z1[1] == tag && z1[3] == tag;
      if (__all(ok) || spin > SPIN_CAP) break;
      __builtin_amdgcn_s_sleep(1);
    }
    sh.big[qq * 64 + 2 * pp] = u2f(z0[0]);
    sh.big[qq * 64 + 2 * pp + 1] = u2f(z0[2]);
    sh.big[(16 + qq) * 64 + 2 * pp] = u2f(z1[0]);
    sh.big[(16 + qq) * 64 + 2 * pp + 1] = u2f(z1[2]);
    wg_barrier();
    if (tid < 64) {
      float s = 0.f;
#pragma unroll
      for (int c = 0; c < NC; ++c) s += sh.big[c * 64 + tid];
      const float o = rbf(rbf(s) + bias2);
      out[col] = f2bf(hv + o);
    }
    if (dj == 0 && tid == 0) __hip_atomic_store(eptr, epoch, __ATOMIC_RELAXED, __HIP_MEMORY_SCOPE_AGENT);
  }
}

extern "C" void kernel_launch(void* const* d_in, const int* in_sizes, int n_in, void* d_out, int out_size, void* d_ws, size_t ws_size,
                              hipStream_t stream) {
  const u16* x = (const u16*)d_in[0];
  const u16* kc = (const u16*)d_in[1];
  const u16* vc = (const u16*)d_in[2];
  const u16* wq = (const u16*)d_in[3];
  const u16* wo = (const u16*)d_in[4];
  const u16* w1 = (const u16*)d_in[6];
  const u16* b1 = (const u16*)d_in[7];
  const u16* w2 = (const u16*)d_in[8];
  const u16* b2 = (const u16*)d_in[9];
  k_fused<<<NWG, 512, 0, stream>>>(x, kc, vc, wq, wo, w1, b1, w2, b2, (u16*)d_out, (unsigned char*)d_ws);
}
```

```cpp
#include <hip/hip_runtime.h>
#include <hip/hip_bf16.h>
#include <stdint.h>

typedef unsigned short u16;
typedef unsigned int u32;
typedef unsigned long long u64;
typedef u32 u32x4 __attribute__((ext_vector_type(4)));
typedef float f32x4 __attribute__((ext_vector_type(4)));
typedef __bf16 bf16x8 __attribute__((ext_vector_type(8)));

constexpr int D_MODEL = 512, HQ = 32, HKV = 8, SK = 65536, DH = 128, FF = 1792;
constexpr int NSPLIT = 32;
constexpr int NWG = HKV * NSPLIT;
constexpr int NB = 32, NC = 28, ND = 8, NTAIL = NB + NC + ND;
constexpr int TILE_EL = 32 * DH;
constexpr float SCALE = 0.08838834764831845f;
constexpr float L2E = 1.4426950408889634f;
constexpr u32 SPIN_CAP = 1u << 18;
constexpr int AUX_SC1 = 16;
#define START 1

constexpr u32 WS_E = 0;
constexpr u32 WS_QG = 256;
constexpr u32 WS_PO = WS_QG + 4096 * 8;
constexpr u32 WS_PML = WS_PO + HQ * NSPLIT * 64 * 16;
constexpr u32 WS_Y = WS_PML + HQ * NSPLIT * 16;
constexpr u32 WS_Z = WS_Y + HQ * 256 * 16;
constexpr u32 WS_END = WS_Z + (NC + 1) * 256 * 16;
#define STAMP(w, i) do { } while (0)

__device__ __forceinline__ float rbf(float x) { return (float)(__bf16)x; }
__device__ __forceinline__ float bf_lo(u32 w) { return __builtin_bit_cast(float, w << 16); }
__device__ __forceinline__ float bf_hi(u32 w) { return __builtin_bit_cast(float, w & 0xffff0000u); }
__device__ __forceinline__ float bf2f(u16 v) { return __builtin_bit_cast(float, (u32)v << 16); }
__device__ __forceinline__ u16 f2bf(float x) { return __builtin_bit_cast(u16, (__bf16)x); }
__device__ __forceinline__ u32 pack_bf(float lo, float hi) { return (u32)f2bf(lo) | ((u32)f2bf(hi) << 16); }
__device__ __forceinline__ u32 f2u(float x) { return __builtin_bit_cast(u32, x); }
__device__ __forceinline__ float u2f(u32 x) { return __builtin_bit_cast(float, x); }

__device__ __forceinline__ f32x4 mfma16(u32x4 a, u32x4 b, f32x4 c) {
  return __builtin_amdgcn_mfma_f32_16x16x32_bf16(__builtin_bit_cast(bf16x8, a), __builtin_bit_cast(bf16x8, b), c, 0, 0, 0);
}
template <int CTRL> __device__ __forceinline__ float dpp(float x) {
  return u2f((u32)__builtin_amdgcn_mov_dpp((int)f2u(x), CTRL, 0xf, 0xf, true));
}
constexpr int XOR1 = 0xB1, XOR2 = 0x4E, XOR7 = 0x141, XOR8 = 0x128;
__device__ __forceinline__ float x16_max(float x) {
  auto s = __builtin_amdgcn_permlane16_swap(f2u(x), f2u(x), false, false);
  return fmaxf(u2f(s[0]), u2f(s[1]));
}
__device__ __forceinline__ float x32_max(float x) {
  auto t = __builtin_amdgcn_permlane32_swap(f2u(x), f2u(x), false, false);
  return fmaxf(u2f(t[0]), u2f(t[1]));
}
__device__ __forceinline__ float x16_sum(float x) {
  auto s = __builtin_amdgcn_permlane16_swap(f2u(x), f2u(x), false, false);
  return u2f(s[0]) + u2f(s[1]);
}
__device__ __forceinline__ float x32_sum(float x) {
  auto t = __builtin_amdgcn_permlane32_swap(f2u(x), f2u(x), false, false);
  return u2f(t[0]) + u2f(t[1]);
}
__device__ __forceinline__ float row_sum(float x) {
  x += dpp<XOR1>(x); x += dpp<XOR2>(x); x += dpp<XOR7>(x); x += dpp<XOR8>(x);
  return x;
}
__device__ __forceinline__ float row_max(float x) {
  x = fmaxf(x, dpp<XOR1>(x)); x = fmaxf(x, dpp<XOR2>(x)); x = fmaxf(x, dpp<XOR7>(x)); x = fmaxf(x, dpp<XOR8>(x));
  return x;
}
__device__ __forceinline__ float half_sum(float x) { return x16_sum(row_sum(x)); }
__device__ __forceinline__ float half_max(float x) { return x16_max(row_max(x)); }
__device__ __forceinline__ float wave_sum(float x) { return x32_sum(half_sum(x)); }

__device__ __forceinline__ void wg_barrier() {
  asm volatile("s_waitcnt lgkmcnt(0)" ::: "memory");
  __builtin_amdgcn_s_barrier();
  asm volatile("" ::: "memory");
}
__device__ __forceinline__ u32x4 ld_sc1(__amdgpu_buffer_rsrc_t rs, u32 off) {
  return __builtin_amdgcn_raw_buffer_load_b128(rs, (int)off, 0, AUX_SC1);
}
__device__ __forceinline__ void st_sc1(__amdgpu_buffer_rsrc_t rs, u32 off, u32x4 v) {
  __builtin_amdgcn_raw_buffer_store_b128(v, rs, (int)off, 0, AUX_SC1);
}
__device__ __forceinline__ void glds16(const void* gsrc, u32 lds_dst) {
  u32 keep;
  asm volatile("s_mov_b32 %0, m0\n\ts_mov_b32 m0, %2\n\ts_nop 0\n\tglobal_load_lds_dwordx4 %1, off\n\ts_mov_b32 m0, %0"
               : "=&s"(keep) : "v"(gsrc), "s"(lds_dst) : "memory");
}

struct Tile { u32x4 k[8]; u32x4 v[8]; };

__device__ __forceinline__ void load_k(Tile& t, const u16* kp, int half) {
#pragma unroll
  for (int c = 0; c < 4; ++c)
    t.k[half * 4 + c] = __builtin_nontemporal_load(reinterpret_cast<const u32x4*>(kp + half * 16 * DH + c * 32));
  __builtin_amdgcn_sched_barrier(0);
}
__device__ __forceinline__ void load_v(Tile& t, const u16* vp, int half) {
#pragma unroll
  for (int i = 0; i < 4; ++i)
    t.v[half * 4 + i] = __builtin_nontemporal_load(reinterpret_cast<const u32x4*>(vp + (half * 16 + i) * DH));
  __builtin_amdgcn_sched_barrier(0);
}
__device__ __forceinline__ void load_tile(Tile& t, const u16* kp, const u16* vp) {
  load_k(t, kp, 0); load_k(t, kp, 1); load_v(t, vp, 0); load_v(t, vp, 1);
}

__device__ __forceinline__ void compute_tile(const Tile& t, const u32x4 (&qf)[4], float& m, float& l, f32x4 (&acc)[8]) {
  f32x4 s0 = {0.f, 0.f, 0.f, 0.f}, s1 = {0.f, 0.f, 0.f, 0.f};
#pragma unroll
  for (int c = 0; c < 4; ++c) {
    s0 = mfma16(t.k[c], qf[c], s0);
    s1 = mfma16(t.k[4 + c], qf[c], s1);
  }
  float sv[8];
#pragma unroll
  for (int r = 0; r < 4; ++r) {
    sv[r] = rbf(rbf(s0[r]) * SCALE);
    sv[4 + r] = rbf(rbf(s1[r]) * SCALE);
  }
  float tm = fmaxf(fmaxf(fmaxf(sv[0], sv[1]), fmaxf(sv[2], sv[3])), fmaxf(fmaxf(sv[4], sv[5]), fmaxf(sv[6], sv[7])));
  tm = x32_max(x16_max(tm));
  if (__any(tm > m)) {
    const float mn = fmaxf(m, tm);
    const float alpha = __builtin_amdgcn_exp2f((m - mn) * L2E);
    m = mn;
    l *= alpha;
#pragma unroll
    for (int j = 0; j < 8; ++j) acc[j] *= alpha;
  }
  float p[8];
#pragma unroll
  for (int i = 0; i < 8; ++i) {
    p[i] = __builtin_amdgcn_exp2f((sv[i] - m) * L2E);
    l += p[i];
  }
  u32x4 pf;
#pragma unroll
  for (int u = 0; u < 4; ++u) pf[u] = pack_bf(p[2 * u], p[2 * u + 1]);
#pragma unroll
  for (int w = 0; w < 4; ++w) {
    u32x4 lo, hi;
#pragma unroll
    for (int u = 0; u < 4; ++u) {
      lo[u] = __builtin_amdgcn_perm(t.v[2 * u + 1][w], t.v[2 * u][w], 0x05040100u);
      hi[u] = __builtin_amdgcn_perm(t.v[2 * u + 1][w], t.v[2 * u][w], 0x07060302u);
    }
    acc[2 * w] = mfma16(lo, pf, acc[2 * w]);
    acc[2 * w + 1] = mfma16(hi, pf, acc[2 * w + 1]);
  }
}

typedef __bf16 bf16x2 __attribute__((ext_vector_type(2)));
__device__ __forceinline__ float dot2(u32 w, u32 a, float c) {
  return __builtin_amdgcn_fdot2_f32_bf16(__builtin_bit_cast(bf16x2, w), __builtin_bit_cast(bf16x2, a), c, false);
}
__device__ __forceinline__ void dot8(float (&acc)[8], u32 alo, u32x4 w) {
  const u32 ahi = alo << 16;
#pragma unroll
  for (int k = 0; k < 4; ++k) {
    acc[2 * k] = dot2(w[k], alo, acc[2 * k]);
    acc[2 * k + 1] = dot2(w[k], ahi, acc[2 * k + 1]);
  }
}
__device__ __forceinline__ float tr32(float a, float b) {
  auto t = __builtin_amdgcn_permlane32_swap(f2u(a), f2u(b), false, false);
  return u2f(t[0]) + u2f(t[1]);
}
__device__ __forceinline__ float tr16(float a, float b) {
  auto t = __builtin_amdgcn_permlane16_swap(f2u(a), f2u(b), false, false);
  return u2f(t[0]) + u2f(t[1]);
}
__device__ __forceinline__ float treduce8_32(const float (&v)[8], int lane) {
  float c[4];
#pragma unroll
  for (int k = 0; k < 4; ++k) c[k] = tr16(v[k], v[k + 4]);
#pragma unroll
  for (int k = 0; k < 4; ++k) c[k] += dpp<XOR8>(c[k]);
  float d0 = (lane & 8) ? c[2] : c[0];
  float d1 = (lane & 8) ? c[3] : c[1];
  d0 += dpp<XOR7>(d0);
  d1 += dpp<XOR7>(d1);
  float e = (lane & 4) ? d1 : d0;
  e += dpp<XOR2>(e);
  e += dpp<XOR1>(e);
  return e;
}
__device__ __forceinline__ float treduce8_64(const float (&v)[8], int lane) {
  float c[4];
#pragma unroll
  for (int k = 0; k < 4; ++k) c[k] = tr32(v[k], v[k + 4]);
  float d0 = tr16(c[0], c[2]), d1 = tr16(c[1], c[3]);
  d0 += dpp<XOR8>(d0);
  d1 += dpp<XOR8>(d1);
  float e = (lane & 8) ? d1 : d0;
  e += dpp<XOR7>(e);
  e += dpp<XOR2>(e);
  e += dpp<XOR1>(e);
  return e;
}
__device__ __forceinline__ float div_rn(float a, float d, float rd) {
  const float q = a * rd;
  return fmaf(fmaf(-q, d, a), rd, q);
}

struct Smem {
  u32x4 wbuf[8192];
  float big[8 * 512];
  float yh[2 * 512];
  u32 lnb[512];
  float sm[8][4], sl[8][4];
  u32 qs[256];
  u64 epoch;
};

__global__ __launch_bounds__(512, 2) void k_fused(const u16* __restrict__ x, const u16* __restrict__ K, const u16* __restrict__ V,
                                                  const u16* __restrict__ wq, const u16* __restrict__ wo, const u16* __restrict__ w1,
                                                  const u16* __restrict__ b1, const u16* __restrict__ w2, const u16* __restrict__ b2,
                                                  u16* __restrict__ out, unsigned char* ws) {
  __shared__ __attribute__((aligned(16))) Smem sh;
  const int tid = threadIdx.x, lane = tid & 63, wave = __builtin_amdgcn_readfirstlane(tid >> 6);
  const int rho = lane & 15, g = lane >> 4;
  const int b = blockIdx.x, kvh = b & 7, sp = b >> 3;
  const __amdgpu_buffer_rsrc_t rs = __builtin_amdgcn_make_buffer_rsrc(ws, 0, (int)WS_END, 0x00020000);
  u64* eptr = reinterpret_cast<u64*>(ws + WS_E);

  const size_t base = ((size_t)kvh * SK + (size_t)sp * 2048 + wave * 32) * DH;
  const u16* kp = K + base + rho * DH + g * 8;
  const u16* vp = V + base + (4 * g) * DH + rho * 8;
  constexpr int TSTRIDE = 8 * TILE_EL;
  const int hb = (b & 7) * 4 + (b >> 3);
  const int cs = b - NB;
  STAMP(0, 0);

  Tile A, B;
  if (wave == 0) {
    const u32x4 zero4 = {0u, 0u, 0u, 0u};
#pragma unroll
    for (int h = 0; h < 4; ++h)
      st_sc1(rs, WS_PO + (u32)((((kvh * 4 + h) * 8 + (lane >> 3)) * NSPLIT + sp) * 8 + (lane & 7)) * 16, zero4);
    if (lane < 4) st_sc1(rs, WS_PML + (u32)((kvh * 4 + lane) * NSPLIT + sp) * 16, zero4);
    if (b < NB) {
#pragma unroll
      for (int i = 0; i < 4; ++i) st_sc1(rs, WS_Y + (u32)(hb * 256 + i * 64 + lane) * 16, zero4);
    } else if (b < NB + NC) {
#pragma unroll
      for (int i = 0; i < 4; ++i) st_sc1(rs, WS_Z + (u32)(cs * 256 + i * 64 + lane) * 16, zero4);
      if (b == NB) {
#pragma unroll
        for (int i = 0; i < 4; ++i) st_sc1(rs, WS_Z + (u32)(NC * 256 + i * 64 + lane) * 16, zero4);
      }
    }
    const u64 epoch = __hip_atomic_load(eptr, __ATOMIC_RELAXED, __HIP_MEMORY_SCOPE_AGENT) + 1ull;
    const int col0 = kvh * 512 + sp * 16;
    const u32x4 xv = *reinterpret_cast<const u32x4*>(x + lane * 8);
    u32x4 wv[16];
#pragma unroll
    for (int i = 0; i < 16; ++i)
      wv[i] = *reinterpret_cast<const u32x4*>(wq + (size_t)((lane & 31) + 32 * i) * 4096 + col0 + (lane >> 5) * 8);
    float xf[8];
#pragma unroll
    for (int k = 0; k < 4; ++k) { xf[2 * k] = bf_lo(xv[k]); xf[2 * k + 1] = bf_hi(xv[k]); }
    float s = 0.f;
#pragma unroll
    for (int k = 0; k < 8; ++k) s += xf[k];
    const float mean = wave_sum(s) * (1.0f / 512.0f);
    float s2 = 0.f;
#pragma unroll
    for (int k = 0; k < 8; ++k) s2 += (xf[k] - mean) * (xf[k] - mean);
    const float var = rbf(wave_sum(s2) * (1.0f / 512.0f));
    const float mb = rbf(mean);
    const float den = rbf(sqrtf(rbf(var + 1e-5f)));
    const float rden = 1.0f / den;
#pragma unroll
    for (int k = 0; k < 8; ++k) sh.lnb[lane * 8 + k] = (u32)f2bf(div_rn(rbf(xf[k] - mb), den, rden));
    asm volatile("s_waitcnt lgkmcnt(0)" ::: "memory");
    float acc[8];
#pragma unroll
    for (int k = 0; k < 8; ++k) acc[k] = 0.f;
#pragma unroll
    for (int i = 0; i < 16; ++i) dot8(acc, sh.lnb[(lane & 31) + 32 * i], wv[i]);
    const float qv1 = treduce8_32(acc, lane);
    const u32 tlo = (u32)(epoch << 16), thi = (u32)(epoch >> 16);
    asm volatile("s_waitcnt vmcnt(0)" ::: "memory");
    if ((lane & 3) == 0)
      __hip_atomic_store(reinterpret_cast<u64*>(ws + WS_QG) + col0 + (lane >> 2), ((u64)thi << 32) | (u64)(tlo | (u32)f2bf(qv1)),
                         __ATOMIC_RELAXED, __HIP_MEMORY_SCOPE_AGENT);
    __builtin_amdgcn_s_barrier();
    STAMP(0, 1);
    const bool tailwg = b < NTAIL;
    const u32 toff0 = WS_QG + (u32)((lane & 7) * 512 + (lane >> 3) * 16) * 8;
    const int b2i = 64 + (lane & 3);
    const u32 toff1 = WS_QG + (u32)((b2i & 7) * 512 + (b2i >> 3) * 16) * 8;
    u32x4 qv[4];
    for (u32 spin = 0;; ++spin) {
      asm volatile("" ::: "memory");
      bool ok = true;
#pragma unroll
      for (int j = 0; j < 4; ++j) qv[j] = ld_sc1(rs, WS_QG + (u32)(kvh * 512) * 8 + (u32)(lane + 64 * j) * 16);
      if (tailwg) {
        const u32x4 t0v = ld_sc1(rs, toff0), t1v = ld_sc1(rs, toff1);
        ok = ((t0v[0] & 0xffff0000u) == tlo) && (t0v[1] == thi) && ((t1v[0] & 0xffff0000u) == tlo) && (t1v[1] == thi);
      }
#pragma unroll
      for (int j = 0; j < 4; ++j)
        ok = ok && ((qv[j][0] & 0xffff0000u) == tlo) && (qv[j][1] == thi) && ((qv[j][2] & 0xffff0000u) == tlo) && (qv[j][3] == thi);
      if (__all(ok) || spin > SPIN_CAP) break;
      __builtin_amdgcn_s_sleep(1);
    }
#pragma unroll
    for (int j = 0; j < 4; ++j) sh.qs[lane + 64 * j] = (qv[j][0] & 0xffffu) | (qv[j][2] << 16);
    if (lane == 0) sh.epoch = epoch;
    STAMP(0, 2);
  } else {
    const u32 wbase = (u32)(size_t)(__attribute__((address_space(3))) void*)sh.wbuf;
    __builtin_amdgcn_s_sleep(2);
    if (b < NB) {
      const u16* src = wo + (size_t)hb * DH * D_MODEL + lane * 8;
      for (int p = wave - 1; p < 128; p += 7) glds16(src + (size_t)p * 512, wbase + (u32)p * 1024);
    } else if (b < NB + NC) {
      const u16* s1 = w1 + (size_t)(lane >> 3) * FF + cs * 64 + (((lane & 7) ^ (lane >> 3)) & 7) * 8;
      const u16* s2 = w2 + (size_t)cs * 64 * D_MODEL + lane * 8;
      for (int p = wave - 1; p < 64; p += 7) {
        glds16(s1 + (size_t)p * 8 * FF, wbase + (u32)p * 1024);
        glds16(s2 + (size_t)p * 512, wbase + 65536u + (u32)p * 1024);
      }
    }
    __builtin_amdgcn_s_barrier();
  }
  load_tile(A, kp, vp);
  load_tile(B, kp + TSTRIDE, vp + TSTRIDE);
  wg_barrier();
  const u64 epoch = sh.epoch;
  const u32 tag = (u32)epoch * 2u + 1u;

  u32x4 qf[4];
#pragma unroll
  for (int c = 0; c < 4; ++c) {
    qf[c] = u32x4{0u, 0u, 0u, 0u};
    if (rho < 4) qf[c] = *reinterpret_cast<const u32x4*>(&sh.qs[rho * 64 + c * 16 + g * 4]);
  }
  float m = -INFINITY, l = 0.f;
  f32x4 acc[8];
#pragma unroll
  for (int j = 0; j < 8; ++j) acc[j] = f32x4{0.f, 0.f, 0.f, 0.f};

#pragma unroll
  for (int it = 0; it < 6; it += 2) {
    compute_tile(A, qf, m, l, acc);
    load_tile(A, kp + (size_t)(it + 2) * TSTRIDE, vp + (size_t)(it + 2) * TSTRIDE);
    compute_tile(B, qf, m, l, acc);
    load_tile(B, kp + (size_t)(it + 3) * TSTRIDE, vp + (size_t)(it + 3) * TSTRIDE);
  }
  compute_tile(A, qf, m, l, acc);
  compute_tile(B, qf, m, l, acc);
  STAMP(0, 3);
  STAMP(7, 4);

  l = x32_sum(x16_sum(l));
  if (rho < 4) {
#pragma unroll
    for (int r = 0; r < 4; ++r) {
      f32x4 a = {acc[0][r], acc[1][r], acc[2][r], acc[3][r]};
      f32x4 c = {acc[4][r], acc[5][r], acc[6][r], acc[7][r]};
      float* dst = &sh.big[(wave * 4 + rho) * DH + 32 * g + 8 * r];
      *reinterpret_cast<f32x4*>(dst) = a;
      *reinterpret_cast<f32x4*>(dst + 4) = c;
    }
    if (g == 0) { sh.sm[wave][rho] = m; sh.sl[wave][rho] = l; }
  }
  asm volatile("s_waitcnt vmcnt(0)" ::: "memory");
  wg_barrier();
  if (tid < 256) {
    const int h = tid >> 6, dp = tid & 63;
    float M = sh.sm[0][h];
#pragma unroll
    for (int w = 1; w < 8; ++w) M = fmaxf(M, sh.sm[w][h]);
    float L = 0.f, O0 = 0.f, O1 = 0.f;
#pragma unroll
    for (int w = 0; w < 8; ++w) {
      const float e = __builtin_amdgcn_exp2f((sh.sm[w][h] - M) * L2E);
      L += e * sh.sl[w][h];
      O0 += e * sh.big[(w * 4 + h) * DH + 2 * dp];
      O1 += e * sh.big[(w * 4 + h) * DH + 2 * dp + 1];
    }
    st_sc1(rs, WS_PO + (u32)((((kvh * 4 + h) * 8 + (dp >> 3)) * NSPLIT + sp) * 8 + (dp & 7)) * 16, u32x4{f2u(O0), tag, f2u(O1), tag});
    if (dp == 0) st_sc1(rs, WS_PML + (u32)((kvh * 4 + h) * NSPLIT + sp) * 16, u32x4{f2u(M), tag, f2u(L), tag});
  }
  STAMP(0, 5);
  if (b >= NTAIL) return;
  wg_barrier();

  if (b < NB) {
    const int s = lane & 31, hf = lane >> 5;
    u32x4 ml, o4[4];
    for (u32 spin = 0;; ++spin) {
      asm volatile("" ::: "memory");
      ml = ld_sc1(rs, WS_PML + (u32)(hb * NSPLIT + s) * 16);
#pragma unroll
      for (int j = 0; j < 4; ++j) o4[j] = ld_sc1(rs, WS_PO + (u32)(((hb * 8 + wave) * NSPLIT + s) * 8 + hf * 4 + j) * 16);
      bool ok = ml[1] == tag && ml[3] == tag;
#pragma unroll
      for (int j = 0; j < 4; ++j) ok = ok && o4[j][1] == tag && o4[j][3] == tag;
      if (__all(ok) || spin > SPIN_CAP) break;
      __builtin_amdgcn_s_sleep(1);
    }
    STAMP(0, 6);
    const float ms = u2f(ml[0]);
    const float M = half_max(ms);
    const float wgt = __builtin_amdgcn_exp2f((ms - M) * L2E);
    const float L = half_sum(wgt * u2f(ml[2]));
    float ow[8];
#pragma unroll
    for (int j = 0; j < 4; ++j) { ow[2 * j] = wgt * u2f(o4[j][0]); ow[2 * j + 1] = wgt * u2f(o4[j][2]); }
    const u32 ob = (u32)f2bf(treduce8_32(ow, lane) / L);
    float a8[8];
#pragma unroll
    for (int k = 0; k < 8; ++k) a8[k] = 0.f;
#pragma unroll
    for (int i = 0; i < 16; ++i) dot8(a8, (u32)__builtin_amdgcn_readlane((int)ob, i * 4), sh.wbuf[(wave * 16 + i) * 64 + lane]);
    *reinterpret_cast<f32x4*>(&sh.big[wave * 512 + lane * 8]) = f32x4{a8[0], a8[1], a8[2], a8[3]};
    *reinterpret_cast<f32x4*>(&sh.big[wave * 512 + lane * 8 + 4]) = f32x4{a8[4], a8[5], a8[6], a8[7]};
    wg_barrier();
    if (tid < 256) {
      float y0 = 0.f, y1 = 0.f;
#pragma unroll
      for (int w = 0; w < 8; ++w) { y0 += sh.big[w * 512 + 2 * tid]; y1 += sh.big[w * 512 + 2 * tid + 1]; }
      st_sc1(rs, WS_Y + (u32)(hb * 256 + tid) * 16, u32x4{f2u(y0), tag, f2u(y1), tag});
    }
    STAMP(0, 7);
  } else if (b < NB + NC) {
    const int cp = tid & 255, hh = tid >> 8;
    float xres[8];
#pragma unroll
    for (int i = 0; i < 8; ++i) xres[i] = bf2f(x[lane + 64 * i]);
    const float bias1 = bf2f(b1[cs * 64 + wave * 8 + (lane >> 3)]);
    u32x4 yv[16];
    for (u32 spin = 0;; ++spin) {
      asm volatile("" ::: "memory");
#pragma unroll
      for (int j = 0; j < 16; ++j) yv[j] = ld_sc1(rs, WS_Y + (u32)((hh * 16 + j) * 256 + cp) * 16);
      bool ok = true;
#pragma unroll
      for (int j = 0; j < 16; ++j) ok = ok && yv[j][1] == tag && yv[j][3] == tag;
      if (__all(ok) || spin > SPIN_CAP) break;
      __builtin_amdgcn_s_sleep(1);
    }
    STAMP(0, 6);
    float s0 = 0.f, s1 = 0.f;
#pragma unroll
    for (int j = 0; j < 16; ++j) { s0 += u2f(yv[j][0]); s1 += u2f(yv[j][2]); }
    *reinterpret_cast<float2*>(&sh.yh[hh * 512 + 2 * cp]) = make_float2(s0, s1);
    wg_barrier();
    float hv[8], hs = 0.f;
#pragma unroll
    for (int i = 0; i < 8; ++i) {
      hv[i] = rbf(xres[i] + rbf(sh.yh[lane + 64 * i] + sh.yh[512 + lane + 64 * i]));
      hs += hv[i];
    }
    if (cs == 0 && wave < 4) {
      const int c0 = wave * 128 + 2 * lane;
      const float h0 = rbf(bf2f(x[c0]) + rbf(sh.yh[c0] + sh.yh[512 + c0]));
      const float h1 = rbf(bf2f(x[c0 + 1]) + rbf(sh.yh[c0 + 1] + sh.yh[512 + c0 + 1]));
      st_sc1(rs, WS_Z + (u32)(NC * 256 + tid) * 16, u32x4{f2u(h0), tag, f2u(h1), tag});
    }
    const float mean = wave_sum(hs) * (1.0f / 512.0f);
    float vs = 0.f;
#pragma unroll
    for (int i = 0; i < 8; ++i) vs += (hv[i] - mean) * (hv[i] - mean);
    const float var = rbf(wave_sum(vs) * (1.0f / 512.0f));
    const float mb = rbf(mean);
    const float den = rbf(sqrtf(rbf(var + 1e-5f)));
    const float rden = 1.0f / den;
    float a8[8];
#pragma unroll
    for (int k = 0; k < 8; ++k) a8[k] = 0.f;
#pragma unroll
    for (int i = 0; i < 8; ++i)
      dot8(a8, (u32)f2bf(div_rn(rbf(hv[i] - mb), den, rden)), sh.wbuf[(lane + 64 * i) * 8 + ((wave ^ lane) & 7)]);
    const float h1 = rbf(rbf(treduce8_64(a8, lane)) + bias1);
    const float rr = h1 > 0.f ? h1 : 0.f;
    const u32 hb16 = (u32)f2bf(rr * rr);
    float z8[8];
#pragma unroll
    for (int k = 0; k < 8; ++k) z8[k] = 0.f;
#pragma unroll
    for (int k = 0; k < 8; ++k) dot8(z8, (u32)__builtin_amdgcn_readlane((int)hb16, k * 8), sh.wbuf[4096 + (wave * 8 + k) * 64 + lane]);
    *reinterpret_cast<f32x4*>(&sh.big[wave * 512 + lane * 8]) = f32x4{z8[0], z8[1], z8[2], z8[3]};
    *reinterpret_cast<f32x4*>(&sh.big[wave * 512 + lane * 8 + 4]) = f32x4{z8[4], z8[5], z8[6], z8[7]};
    wg_barrier();
    if (tid < 256) {
      float z0 = 0.f, z1 = 0.f;
#pragma unroll
      for (int w = 0; w < 8; ++w) { z0 += sh.big[w * 512 + 2 * tid]; z1 += sh.big[w * 512 + 2 * tid + 1]; }
      st_sc1(rs, WS_Z + (u32)(cs * 256 + tid) * 16, u32x4{f2u(z0), tag, f2u(z1), tag});
    }
    STAMP(0, 7);
  } else {
    const int dj = b - NB - NC;
    const int c = lane & 31, pr = lane >> 5;
    const int cc = c <= NC ? c : NC;
    const int col = dj * 64 + wave * 8 + pr * 4;
    const uint2 bias2 = *reinterpret_cast<const uint2*>(b2 + col);
    u32x4 z0, z1;
    for (u32 spin = 0;; ++spin) {
      asm volatile("" ::: "memory");
      z0 = ld_sc1(rs, WS_Z + (u32)(cc * 256 + (col >> 1)) * 16);
      z1 = ld_sc1(rs, WS_Z + (u32)(cc * 256 + (col >> 1) + 1) * 16);
      const bool ok = z0[1] == tag && z0[3] == tag && z1[1] == tag && z1[3] == tag;
      if (__all(ok) || spin > SPIN_CAP) break;
      __builtin_amdgcn_s_sleep(1);
    }
    STAMP(0, 6);
    float v[4] = {u2f(z0[0]), u2f(z0[2]), u2f(z1[0]), u2f(z1[2])};
    const float bb[4] = {bf_lo(bias2.x), bf_hi(bias2.x), bf_lo(bias2.y), bf_hi(bias2.y)};
    float r[4];
#pragma unroll
    for (int k = 0; k < 4; ++k) {
      const float hres = __shfl(v[k], NC + 32 * pr);
      const float zs = half_sum(c < NC ? v[k] : 0.f);
      r[k] = rbf(hres + rbf(rbf(zs) + bb[k]));
    }
    if (c == 0) *reinterpret_cast<uint2*>(out + col) = make_uint2(pack_bf(r[0], r[1]), pack_bf(r[2], r[3]));
    STAMP(0, 7);
    if (dj == 0 && tid == 0) __hip_atomic_store(eptr, epoch, __ATOMIC_RELAXED, __HIP_MEMORY_SCOPE_AGENT);
  }
}

extern "C" void kernel_launch(void* const* d_in, const int* in_sizes, int n_in, void* d_out, int out_size, void* d_ws, size_t ws_size,
                              hipStream_t stream) {
  const u16* x = (const u16*)d_in[0];
  const u16* kc = (const u16*)d_in[1];
  const u16* vc = (const u16*)d_in[2];
  const u16* wq = (const u16*)d_in[3];
  const u16* wo = (const u16*)d_in[4];
  const u16* w1 = (const u16*)d_in[6];
  const u16* b1 = (const u16*)d_in[7];
  const u16* w2 = (const u16*)d_in[8];
  const u16* b2 = (const u16*)d_in[9];
  k_fused<<<NWG, 512, 0, stream>>>(x, kc, vc, wq, wo, w1, b1, w2, b2, (u16*)d_out, (unsigned char*)d_ws);
}
```

```cpp
#include <hip/hip_runtime.h>
#include <hip/hip_bf16.h>
#include <stdint.h>

typedef unsigned short u16;
typedef unsigned int u32;
typedef unsigned long long u64;
typedef u32 u32x4 __attribute__((ext_vector_type(4)));
typedef float f32x4 __attribute__((ext_vector_type(4)));
typedef __bf16 bf16x8 __attribute__((ext_vector_type(8)));

constexpr int D_MODEL = 512, HQ = 32, HKV = 8, SK = 65536, DH = 128, FF = 1792;
constexpr int NSPLIT = 32;
constexpr int NWG = HKV * NSPLIT;
constexpr int NB = 32, NC = 28, ND = 8, NTAIL = NB + NC + ND;
constexpr int TILE_EL = 32 * DH;
constexpr float SCALE = 0.08838834764831845f;
constexpr float L2E = 1.4426950408889634f;
constexpr u32 SPIN_CAP = 1u << 18;
constexpr int AUX_SC1 = 16;
constexpr int KV_AUX = 18;
#define START 1

constexpr u32 WS_E = 0;
constexpr u32 WS_QG = 256;
constexpr u32 WS_PO = WS_QG + 4096 * 8;
constexpr u32 WS_PML = WS_PO + HQ * NSPLIT * 64 * 16;
constexpr u32 WS_Y = WS_PML + HQ * NSPLIT * 16;
constexpr u32 WS_Z = WS_Y + HQ * 256 * 16;
constexpr u32 WS_END = WS_Z + (NC + 1) * 256 * 16;
#define STAMP(w, i) do { } while (0)

__device__ __forceinline__ float rbf(float x) { return (float)(__bf16)x; }
__device__ __forceinline__ float bf_lo(u32 w) { return __builtin_bit_cast(float, w << 16); }
__device__ __forceinline__ float bf_hi(u32 w) { return __builtin_bit_cast(float, w & 0xffff0000u); }
__device__ __forceinline__ float bf2f(u16 v) { return __builtin_bit_cast(float, (u32)v << 16); }
__device__ __forceinline__ u16 f2bf(float x) { return __builtin_bit_cast(u16, (__bf16)x); }
__device__ __forceinline__ u32 pack_bf(float lo, float hi) { return (u32)f2bf(lo) | ((u32)f2bf(hi) << 16); }
__device__ __forceinline__ u32 f2u(float x) { return __builtin_bit_cast(u32, x); }
__device__ __forceinline__ float u2f(u32 x) { return __builtin_bit_cast(float, x); }

__device__ __forceinline__ f32x4 mfma16(u32x4 a, u32x4 b, f32x4 c) {
  return __builtin_amdgcn_mfma_f32_16x16x32_bf16(__builtin_bit_cast(bf16x8, a), __builtin_bit_cast(bf16x8, b), c, 0, 0, 0);
}
template <int CTRL> __device__ __forceinline__ float dpp(float x) {
  return u2f((u32)__builtin_amdgcn_mov_dpp((int)f2u(x), CTRL, 0xf, 0xf, true));
}
constexpr int XOR1 = 0xB1, XOR2 = 0x4E, XOR7 = 0x141, XOR8 = 0x128;
__device__ __forceinline__ float x16_max(float x) {
  auto s = __builtin_amdgcn_permlane16_swap(f2u(x), f2u(x), false, false);
  return fmaxf(u2f(s[0]), u2f(s[1]));
}
__device__ __forceinline__ float x32_max(float x) {
  auto t = __builtin_amdgcn_permlane32_swap(f2u(x), f2u(x), false, false);
  return fmaxf(u2f(t[0]), u2f(t[1]));
}
__device__ __forceinline__ float x16_sum(float x) {
  auto s = __builtin_amdgcn_permlane16_swap(f2u(x), f2u(x), false, false);
  return u2f(s[0]) + u2f(s[1]);
}
__device__ __forceinline__ float x32_sum(float x) {
  auto t = __builtin_amdgcn_permlane32_swap(f2u(x), f2u(x), false, false);
  return u2f(t[0]) + u2f(t[1]);
}
__device__ __forceinline__ float row_sum(float x) {
  x += dpp<XOR1>(x); x += dpp<XOR2>(x); x += dpp<XOR7>(x); x += dpp<XOR8>(x);
  return x;
}
__device__ __forceinline__ float row_max(float x) {
  x = fmaxf(x, dpp<XOR1>(x)); x = fmaxf(x, dpp<XOR2>(x)); x = fmaxf(x, dpp<XOR7>(x)); x = fmaxf(x, dpp<XOR8>(x));
  return x;
}
__device__ __forceinline__ float half_sum(float x) { return x16_sum(row_sum(x)); }
__device__ __forceinline__ float half_max(float x) { return x16_max(row_max(x)); }
__device__ __forceinline__ float wave_sum(float x) { return x32_sum(half_sum(x)); }

__device__ __forceinline__ void wg_barrier() {
  asm volatile("s_waitcnt lgkmcnt(0)" ::: "memory");
  __builtin_amdgcn_s_barrier();
  asm volatile("" ::: "memory");
}
__device__ __forceinline__ u32x4 ld_sc1(__amdgpu_buffer_rsrc_t rs, u32 off) {
  return __builtin_amdgcn_raw_buffer_load_b128(rs, (int)off, 0, AUX_SC1);
}
__device__ __forceinline__ void st_sc1(__amdgpu_buffer_rsrc_t rs, u32 off, u32x4 v) {
  __builtin_amdgcn_raw_buffer_store_b128(v, rs, (int)off, 0, AUX_SC1);
}
__device__ __forceinline__ void glds16(const void* gsrc, u32 lds_dst) {
  u32 keep;
  asm volatile("s_mov_b32 %0, m0\n\ts_mov_b32 m0, %2\n\ts_nop 0\n\tglobal_load_lds_dwordx4 %1, off\n\ts_mov_b32 m0, %0"
               : "=&s"(keep) : "v"(gsrc), "s"(lds_dst) : "memory");
}

struct Tile { u32x4 k[8]; u32x4 v[8]; };

__device__ __forceinline__ u32x4 ldkv(__amdgpu_buffer_rsrc_t r, const u16* base, const u16* p) {
  return __builtin_amdgcn_raw_buffer_load_b128(r, (int)(u32)((const char*)p - (const char*)base), 0, KV_AUX);
}
__device__ __forceinline__ void load_k(Tile& t, const u16* kp, int half, __amdgpu_buffer_rsrc_t rK, const u16* K0) {
#pragma unroll
  for (int c = 0; c < 4; ++c)
    t.k[half * 4 + c] = ldkv(rK, K0, kp + half * 16 * DH + c * 32);
  __builtin_amdgcn_sched_barrier(0);
}
__device__ __forceinline__ void load_v(Tile& t, const u16* vp, int half, __amdgpu_buffer_rsrc_t rV, const u16* V0) {
#pragma unroll
  for (int i = 0; i < 4; ++i)
    t.v[half * 4 + i] = ldkv(rV, V0, vp + (half * 16 + i) * DH);
  __builtin_amdgcn_sched_barrier(0);
}
#define load_tile(t, kp_, vp_) do { load_k(t, kp_, 0, rK, K); load_k(t, kp_, 1, rK, K); load_v(t, vp_, 0, rV, V); load_v(t, vp_, 1, rV, V); } while (0)

__device__ __forceinline__ void compute_tile(const Tile& t, const u32x4 (&qf)[4], float& m, float& l, f32x4 (&acc)[8]) {
  f32x4 s0 = {0.f, 0.f, 0.f, 0.f}, s1 = {0.f, 0.f, 0.f, 0.f};
#pragma unroll
  for (int c = 0; c < 4; ++c) {
    s0 = mfma16(t.k[c], qf[c], s0);
    s1 = mfma16(t.k[4 + c], qf[c], s1);
  }
  float sv[8];
#pragma unroll
  for (int r = 0; r < 4; ++r) {
    sv[r] = rbf(rbf(s0[r]) * SCALE);
    sv[4 + r] = rbf(rbf(s1[r]) * SCALE);
  }
  float tm = fmaxf(fmaxf(fmaxf(sv[0], sv[1]), fmaxf(sv[2], sv[3])), fmaxf(fmaxf(sv[4], sv[5]), fmaxf(sv[6], sv[7])));
  tm = x32_max(x16_max(tm));
  if (__any(tm > m)) {
    const float mn = fmaxf(m, tm);
    const float alpha = __builtin_amdgcn_exp2f((m - mn) * L2E);
    m = mn;
    l *= alpha;
#pragma unroll
    for (int j = 0; j < 8; ++j) acc[j] *= alpha;
  }
  float p[8];
#pragma unroll
  for (int i = 0; i < 8; ++i) {
    p[i] = __builtin_amdgcn_exp2f((sv[i] - m) * L2E);
    l += p[i];
  }
  u32x4 pf;
#pragma unroll
  for (int u = 0; u < 4; ++u) pf[u] = pack_bf(p[2 * u], p[2 * u + 1]);
#pragma unroll
  for (int w = 0; w < 4; ++w) {
    u32x4 lo, hi;
#pragma unroll
    for (int u = 0; u < 4; ++u) {
      lo[u] = __builtin_amdgcn_perm(t.v[2 * u + 1][w], t.v[2 * u][w], 0x05040100u);
      hi[u] = __builtin_amdgcn_perm(t.v[2 * u + 1][w], t.v[2 * u][w], 0x07060302u);
    }
    acc[2 * w] = mfma16(lo, pf, acc[2 * w]);
    acc[2 * w + 1] = mfma16(hi, pf, acc[2 * w + 1]);
  }
}

typedef __bf16 bf16x2 __attribute__((ext_vector_type(2)));
__device__ __forceinline__ float dot2(u32 w, u32 a, float c) {
  return __builtin_amdgcn_fdot2_f32_bf16(__builtin_bit_cast(bf16x2, w), __builtin_bit_cast(bf16x2, a), c, false);
}
__device__ __forceinline__ void dot8(float (&acc)[8], u32 alo, u32x4 w) {
  const u32 ahi = alo << 16;
#pragma unroll
  for (int k = 0; k < 4; ++k) {
    acc[2 * k] = dot2(w[k], alo, acc[2 * k]);
    acc[2 * k + 1] = dot2(w[k], ahi, acc[2 * k + 1]);
  }
}
__device__ __forceinline__ float tr32(float a, float b) {
  auto t = __builtin_amdgcn_permlane32_swap(f2u(a), f2u(b), false, false);
  return u2f(t[0]) + u2f(t[1]);
}
__device__ __forceinline__ float tr16(float a, float b) {
  auto t = __builtin_amdgcn_permlane16_swap(f2u(a), f2u(b), false, false);
  return u2f(t[0]) + u2f(t[1]);
}
__device__ __forceinline__ float treduce8_32(const float (&v)[8], int lane) {
  float c[4];
#pragma unroll
  for (int k = 0; k < 4; ++k) c[k] = tr16(v[k], v[k + 4]);
#pragma unroll
  for (int k = 0; k < 4; ++k) c[k] += dpp<XOR8>(c[k]);
  float d0 = (lane & 8) ? c[2] : c[0];
  float d1 = (lane & 8) ? c[3] : c[1];
  d0 += dpp<XOR7>(d0);
  d1 += dpp<XOR7>(d1);
  float e = (lane & 4) ? d1 : d0;
  e += dpp<XOR2>(e);
  e += dpp<XOR1>(e);
  return e;
}
__device__ __forceinline__ float treduce8_64(const float (&v)[8], int lane) {
  float c[4];
#pragma unroll
  for (int k = 0; k < 4; ++k) c[k] = tr32(v[k], v[k + 4]);
  float d0 = tr16(c[0], c[2]), d1 = tr16(c[1], c[3]);
  d0 += dpp<XOR8>(d0);
  d1 += dpp<XOR8>(d1);
  float e = (lane & 8) ? d1 : d0;
  e += dpp<XOR7>(e);
  e += dpp<XOR2>(e);
  e += dpp<XOR1>(e);
  return e;
}
__device__ __forceinline__ float div_rn(float a, float d, float rd) {
  const float q = a * rd;
  return fmaf(fmaf(-q, d, a), rd, q);
}

struct Smem {
  u32x4 wbuf[8192];
  float big[8 * 512];
  float yh[2 * 512];
  u32 lnb[512];
  float sm[8][4], sl[8][4];
  u32 qs[256];
  u64 epoch;
};

__global__ __launch_bounds__(512, 2) void k_fused(const u16* __restrict__ x, const u16* __restrict__ K, const u16* __restrict__ V,
                                                  const u16* __restrict__ wq, const u16* __restrict__ wo, const u16* __restrict__ w1,
                                                  const u16* __restrict__ b1, const u16* __restrict__ w2, const u16* __restrict__ b2,
                                                  u16* __restrict__ out, unsigned char* ws) {
  __shared__ __attribute__((aligned(16))) Smem sh;
  const int tid = threadIdx.x, lane = tid & 63, wave = __builtin_amdgcn_readfirstlane(tid >> 6);
  const int rho = lane & 15, g = lane >> 4;
  const int b = blockIdx.x, kvh = b & 7, sp = b >> 3;
  const __amdgpu_buffer_rsrc_t rs = __builtin_amdgcn_make_buffer_rsrc(ws, 0, (int)WS_END, 0x00020000);
  u64* eptr = reinterpret_cast<u64*>(ws + WS_E);
  const __amdgpu_buffer_rsrc_t rK = __builtin_amdgcn_make_buffer_rsrc((void*)K, 0, 134217728, 0x00020000);
  const __amdgpu_buffer_rsrc_t rV = __builtin_amdgcn_make_buffer_rsrc((void*)V, 0, 134217728, 0x00020000);

  const size_t base = ((size_t)kvh * SK + (size_t)sp * 2048 + wave * 32) * DH;
  const u16* kp = K + base + rho * DH + g * 8;
  const u16* vp = V + base + (4 * g) * DH + rho * 8;
  constexpr int TSTRIDE = 8 * TILE_EL;
  const int hb = (b & 7) * 4 + (b >> 3);
  const int cs = b - NB;
  STAMP(0, 0);

  Tile A, B;
  if (wave == 0) {
    const u32x4 zero4 = {0u, 0u, 0u, 0u};
#pragma unroll
    for (int h = 0; h < 4; ++h)
      st_sc1(rs, WS_PO + (u32)((((kvh * 4 + h) * 8 + (lane >> 3)) * NSPLIT + sp) * 8 + (lane & 7)) * 16, zero4);
    if (lane < 4) st_sc1(rs, WS_PML + (u32)((kvh * 4 + lane) * NSPLIT + sp) * 16, zero4);
    if (b < NB) {
#pragma unroll
      for (int i = 0; i < 4; ++i) st_sc1(rs, WS_Y + (u32)(hb * 256 + i * 64 + lane) * 16, zero4);
    } else if (b < NB + NC) {
#pragma unroll
      for (int i = 0; i < 4; ++i) st_sc1(rs, WS_Z + (u32)(cs * 256 + i * 64 + lane) * 16, zero4);
      if (b == NB) {
#pragma unroll
        for (int i = 0; i < 4; ++i) st_sc1(rs, WS_Z + (u32)(NC * 256 + i * 64 + lane) * 16, zero4);
      }
    }
    const u64 epoch = __hip_atomic_load(eptr, __ATOMIC_RELAXED, __HIP_MEMORY_SCOPE_AGENT) + 1ull;
    const int col0 = kvh * 512 + sp * 16;
    const u32x4 xv = *reinterpret_cast<const u32x4*>(x + lane * 8);
    u32x4 wv[16];
#pragma unroll
    for (int i = 0; i < 16; ++i)
      wv[i] = *reinterpret_cast<const u32x4*>(wq + (size_t)((lane & 31) + 32 * i) * 4096 + col0 + (lane >> 5) * 8);
    float xf[8];
#pragma unroll
    for (int k = 0; k < 4; ++k) { xf[2 * k] = bf_lo(xv[k]); xf[2 * k + 1] = bf_hi(xv[k]); }
    float s = 0.f;
#pragma unroll
    for (int k = 0; k < 8; ++k) s += xf[k];
    const float mean = wave_sum(s) * (1.0f / 512.0f);
    float s2 = 0.f;
#pragma unroll
    for (int k = 0; k < 8; ++k) s2 += (xf[k] - mean) * (xf[k] - mean);
    const float var = rbf(wave_sum(s2) * (1.0f / 512.0f));
    const float mb = rbf(mean);
    const float den = rbf(sqrtf(rbf(var + 1e-5f)));
    const float rden = 1.0f / den;
#pragma unroll
    for (int k = 0; k < 8; ++k) sh.lnb[lane * 8 + k] = (u32)f2bf(div_rn(rbf(xf[k] - mb), den, rden));
    asm volatile("s_waitcnt lgkmcnt(0)" ::: "memory");
    float acc[8];
#pragma unroll
    for (int k = 0; k < 8; ++k) acc[k] = 0.f;
#pragma unroll
    for (int i = 0; i < 16; ++i) dot8(acc, sh.lnb[(lane & 31) + 32 * i], wv[i]);
    const float qv1 = treduce8_32(acc, lane);
    const u32 tlo = (u32)(epoch << 16), thi = (u32)(epoch >> 16);
    asm volatile("s_waitcnt vmcnt(0)" ::: "memory");
    if ((lane & 3) == 0)
      __hip_atomic_store(reinterpret_cast<u64*>(ws + WS_QG) + col0 + (lane >> 2), ((u64)thi << 32) | (u64)(tlo | (u32)f2bf(qv1)),
                         __ATOMIC_RELAXED, __HIP_MEMORY_SCOPE_AGENT);
    __builtin_amdgcn_s_barrier();
    STAMP(0, 1);
    const bool tailwg = b < NTAIL;
    const u32 toff0 = WS_QG + (u32)((lane & 7) * 512 + (lane >> 3) * 16) * 8;
    const int b2i = 64 + (lane & 3);
    const u32 toff1 = WS_QG + (u32)((b2i & 7) * 512 + (b2i >> 3) * 16) * 8;
    u32x4 qv[4];
    for (u32 spin = 0;; ++spin) {
      asm volatile("" ::: "memory");
      bool ok = true;
#pragma unroll
      for (int j = 0; j < 4; ++j) qv[j] = ld_sc1(rs, WS_QG + (u32)(kvh * 512) * 8 + (u32)(lane + 64 * j) * 16);
      if (tailwg) {
        const u32x4 t0v = ld_sc1(rs, toff0), t1v = ld_sc1(rs, toff1);
        ok = ((t0v[0] & 0xffff0000u) == tlo) && (t0v[1] == thi) && ((t1v[0] & 0xffff0000u) == tlo) && (t1v[1] == thi);
      }
#pragma unroll
      for (int j = 0; j < 4; ++j)
        ok = ok && ((qv[j][0] & 0xffff0000u) == tlo) && (qv[j][1] == thi) && ((qv[j][2] & 0xffff0000u) == tlo) && (qv[j][3] == thi);
      if (__all(ok) || spin > SPIN_CAP) break;
      __builtin_amdgcn_s_sleep(1);
    }
#pragma unroll
    for (int j = 0; j < 4; ++j) sh.qs[lane + 64 * j] = (qv[j][0] & 0xffffu) | (qv[j][2] << 16);
    if (lane == 0) sh.epoch = epoch;
    STAMP(0, 2);
  } else {
    const u32 wbase = (u32)(size_t)(__attribute__((address_space(3))) void*)sh.wbuf;
    __builtin_amdgcn_s_sleep(2);
    if (b < NB) {
      const u16* src = wo + (size_t)hb * DH * D_MODEL + lane * 8;
      for (int p = wave - 1; p < 128; p += 7) glds16(src + (size_t)p * 512, wbase + (u32)p * 1024);
    } else if (b < NB + NC) {
      const u16* s1 = w1 + (size_t)(lane >> 3) * FF + cs * 64 + (((lane & 7) ^ (lane >> 3)) & 7) * 8;
      const u16* s2 = w2 + (size_t)cs * 64 * D_MODEL + lane * 8;
      for (int p = wave - 1; p < 64; p += 7) {
        glds16(s1 + (size_t)p * 8 * FF, wbase + (u32)p * 1024);
        glds16(s2 + (size_t)p * 512, wbase + 65536u + (u32)p * 1024);
      }
    }
    __builtin_amdgcn_s_barrier();
  }
  load_tile(A, kp, vp);
  load_tile(B, kp + TSTRIDE, vp + TSTRIDE);
  wg_barrier();
  const u64 epoch = sh.epoch;
  const u32 tag = (u32)epoch * 2u + 1u;

  u32x4 qf[4];
#pragma unroll
  for (int c = 0; c < 4; ++c) {
    qf[c] = u32x4{0u, 0u, 0u, 0u};
    if (rho < 4) qf[c] = *reinterpret_cast<const u32x4*>(&sh.qs[rho * 64 + c * 16 + g * 4]);
  }
  float m = -INFINITY, l = 0.f;
  f32x4 acc[8];
#pragma unroll
  for (int j = 0; j < 8; ++j) acc[j] = f32x4{0.f, 0.f, 0.f, 0.f};

#pragma unroll
  for (int it = 0; it < 6; it += 2) {
    compute_tile(A, qf, m, l, acc);
    load_tile(A, kp + (size_t)(it + 2) * TSTRIDE, vp + (size_t)(it + 2) * TSTRIDE);
    compute_tile(B, qf, m, l, acc);
    load_tile(B, kp + (size_t)(it + 3) * TSTRIDE, vp + (size_t)(it + 3) * TSTRIDE);
  }
  compute_tile(A, qf, m, l, acc);
  compute_tile(B, qf, m, l, acc);
  STAMP(0, 3);
  STAMP(7, 4);

  l = x32_sum(x16_sum(l));
  if (rho < 4) {
#pragma unroll
    for (int r = 0; r < 4; ++r) {
      f32x4 a = {acc[0][r], acc[1][r], acc[2][r], acc[3][r]};
      f32x4 c = {acc[4][r], acc[5][r], acc[6][r], acc[7][r]};
      float* dst = &sh.big[(wave * 4 + rho) * DH + 32 * g + 8 * r];
      *reinterpret_cast<f32x4*>(dst) = a;
      *reinterpret_cast<f32x4*>(dst + 4) = c;
    }
    if (g == 0) { sh.sm[wave][rho] = m; sh.sl[wave][rho] = l; }
  }
  asm volatile("s_waitcnt vmcnt(0)" ::: "memory");
  wg_barrier();
  if (tid < 256) {
    const int h = tid >> 6, dp = tid & 63;
    float M = sh.sm[0][h];
#pragma unroll
    for (int w = 1; w < 8; ++w) M = fmaxf(M, sh.sm[w][h]);
    float L = 0.f, O0 = 0.f, O1 = 0.f;
#pragma unroll
    for (int w = 0; w < 8; ++w) {
      const float e = __builtin_amdgcn_exp2f((sh.sm[w][h] - M) * L2E);
      L += e * sh.sl[w][h];
      O0 += e * sh.big[(w * 4 + h) * DH + 2 * dp];
      O1 += e * sh.big[(w * 4 + h) * DH + 2 * dp + 1];
    }
    st_sc1(rs, WS_PO + (u32)((((kvh * 4 + h) * 8 + (dp >> 3)) * NSPLIT + sp) * 8 + (dp & 7)) * 16, u32x4{f2u(O0), tag, f2u(O1), tag});
    if (dp == 0) st_sc1(rs, WS_PML + (u32)((kvh * 4 + h) * NSPLIT + sp) * 16, u32x4{f2u(M), tag, f2u(L), tag});
  }
  STAMP(0, 5);
  if (b >= NTAIL) return;
  wg_barrier();

  if (b < NB) {
    const int s = lane & 31, hf = lane >> 5;
    u32x4 ml, o4[4];
    for (u32 spin = 0;; ++spin) {
      asm volatile("" ::: "memory");
      ml = ld_sc1(rs, WS_PML + (u32)(hb * NSPLIT + s) * 16);
#pragma unroll
      for (int j = 0; j < 4; ++j) o4[j] = ld_sc1(rs, WS_PO + (u32)(((hb * 8 + wave) * NSPLIT + s) * 8 + hf * 4 + j) * 16);
      bool ok = ml[1] == tag && ml[3] == tag;
#pragma unroll
      for (int j = 0; j < 4; ++j) ok = ok && o4[j][1] == tag && o4[j][3] == tag;
      if (__all(ok) || spin > SPIN_CAP) break;
      __builtin_amdgcn_s_sleep(1);
    }
    STAMP(0, 6);
    const float ms = u2f(ml[0]);
    const float M = half_max(ms);
    const float wgt = __builtin_amdgcn_exp2f((ms - M) * L2E);
    const float L = half_sum(wgt * u2f(ml[2]));
    float ow[8];
#pragma unroll
    for (int j = 0; j < 4; ++j) { ow[2 * j] = wgt * u2f(o4[j][0]); ow[2 * j + 1] = wgt * u2f(o4[j][2]); }
    const u32 ob = (u32)f2bf(treduce8_32(ow, lane) / L);
    float a8[8];
#pragma unroll
    for (int k = 0; k < 8; ++k) a8[k] = 0.f;
#pragma unroll
    for (int i = 0; i < 16; ++i) dot8(a8, (u32)__builtin_amdgcn_readlane((int)ob, i * 4), sh.wbuf[(wave * 16 + i) * 64 + lane]);
    *reinterpret_cast<f32x4*>(&sh.big[wave * 512 + lane * 8]) = f32x4{a8[0], a8[1], a8[2], a8[3]};
    *reinterpret_cast<f32x4*>(&sh.big[wave * 512 + lane * 8 + 4]) = f32x4{a8[4], a8[5], a8[6], a8[7]};
    wg_barrier();
    if (tid < 256) {
      float y0 = 0.f, y1 = 0.f;
#pragma unroll
      for (int w = 0; w < 8; ++w) { y0 += sh.big[w * 512 + 2 * tid]; y1 += sh.big[w * 512 + 2 * tid + 1]; }
      st_sc1(rs, WS_Y + (u32)(hb * 256 + tid) * 16, u32x4{f2u(y0), tag, f2u(y1), tag});
    }
    STAMP(0, 7);
  } else if (b < NB + NC) {
    const int cp = tid & 255, hh = tid >> 8;
    float xres[8];
#pragma unroll
    for (int i = 0; i < 8; ++i) xres[i] = bf2f(x[lane + 64 * i]);
    const float bias1 = bf2f(b1[cs * 64 + wave * 8 + (lane >> 3)]);
    u32x4 yv[16];
    for (u32 spin = 0;; ++spin) {
      asm volatile("" ::: "memory");
#pragma unroll
      for (int j = 0; j < 16; ++j) yv[j] = ld_sc1(rs, WS_Y + (u32)((hh * 16 + j) * 256 + cp) * 16);
      bool ok = true;
#pragma unroll
      for (int j = 0; j < 16; ++j) ok = ok && yv[j][1] == tag && yv[j][3] == tag;
      if (__all(ok) || spin > SPIN_CAP) break;
      __builtin_amdgcn_s_sleep(1);
    }
    STAMP(0, 6);
    float s0 = 0.f, s1 = 0.f;
#pragma unroll
    for (int j = 0; j < 16; ++j) { s0 += u2f(yv[j][0]); s1 += u2f(yv[j][2]); }
    *reinterpret_cast<float2*>(&sh.yh[hh * 512 + 2 * cp]) = make_float2(s0, s1);
    wg_barrier();
    float hv[8], hs = 0.f;
#pragma unroll
    for (int i = 0; i < 8; ++i) {
      hv[i] = rbf(xres[i] + rbf(sh.yh[lane + 64 * i] + sh.yh[512 + lane + 64 * i]));
      hs += hv[i];
    }
    if (cs == 0 && wave < 4) {
      const int c0 = wave * 128 + 2 * lane;
      const float h0 = rbf(bf2f(x[c0]) + rbf(sh.yh[c0] + sh.yh[512 + c0]));
      const float h1 = rbf(bf2f(x[c0 + 1]) + rbf(sh.yh[c0 + 1] + sh.yh[512 + c0 + 1]));
      st_sc1(rs, WS_Z + (u32)(NC * 256 + tid) * 16, u32x4{f2u(h0), tag, f2u(h1), tag});
    }
    const float mean = wave_sum(hs) * (1.0f / 512.0f);
    float vs = 0.f;
#pragma unroll
    for (int i = 0; i < 8; ++i) vs += (hv[i] - mean) * (hv[i] - mean);
    const float var = rbf(wave_sum(vs) * (1.0f / 512.0f));
    const float mb = rbf(mean);
    const float den = rbf(sqrtf(rbf(var + 1e-5f)));
    const float rden = 1.0f / den;
    float a8[8];
#pragma unroll
    for (int k = 0; k < 8; ++k) a8[k] = 0.f;
#pragma unroll
    for (int i = 0; i < 8; ++i)
      dot8(a8, (u32)f2bf(div_rn(rbf(hv[i] - mb), den, rden)), sh.wbuf[(lane + 64 * i) * 8 + ((wave ^ lane) & 7)]);
    const float h1 = rbf(rbf(treduce8_64(a8, lane)) + bias1);
    const float rr = h1 > 0.f ? h1 : 0.f;
    const u32 hb16 = (u32)f2bf(rr * rr);
    float z8[8];
#pragma unroll
    for (int k = 0; k < 8; ++k) z8[k] = 0.f;
#pragma unroll
    for (int k = 0; k < 8; ++k) dot8(z8, (u32)__builtin_amdgcn_readlane((int)hb16, k * 8), sh.wbuf[4096 + (wave * 8 + k) * 64 + lane]);
    *reinterpret_cast<f32x4*>(&sh.big[wave * 512 + lane * 8]) = f32x4{z8[0], z8[1], z8[2], z8[3]};
    *reinterpret_cast<f32x4*>(&sh.big[wave * 512 + lane * 8 + 4]) = f32x4{z8[4], z8[5], z8[6], z8[7]};
    wg_barrier();
    if (tid < 256) {
      float z0 = 0.f, z1 = 0.f;
#pragma unroll
      for (int w = 0; w < 8; ++w) { z0 += sh.big[w * 512 + 2 * tid]; z1 += sh.big[w * 512 + 2 * tid + 1]; }
      st_sc1(rs, WS_Z + (u32)(cs * 256 + tid) * 16, u32x4{f2u(z0), tag, f2u(z1), tag});
    }
    STAMP(0, 7);
  } else {
    const int dj = b - NB - NC;
    const int c = lane & 31, pr = lane >> 5;
    const int cc = c <= NC ? c : NC;
    const int col = dj * 64 + wave * 8 + pr * 4;
    const uint2 bias2 = *reinterpret_cast<const uint2*>(b2 + col);
    u32x4 z0, z1;
    for (u32 spin = 0;; ++spin) {
      asm volatile("" ::: "memory");
      z0 = ld_sc1(rs, WS_Z + (u32)(cc * 256 + (col >> 1)) * 16);
      z1 = ld_sc1(rs, WS_Z + (u32)(cc * 256 + (col >> 1) + 1) * 16);
      const bool ok = z0[1] == tag && z0[3] == tag && z1[1] == tag && z1[3] == tag;
      if (__all(ok) || spin > SPIN_CAP) break;
      __builtin_amdgcn_s_sleep(1);
    }
    STAMP(0, 6);
    float v[4] = {u2f(z0[0]), u2f(z0[2]), u2f(z1[0]), u2f(z1[2])};
    const float bb[4] = {bf_lo(bias2.x), bf_hi(bias2.x), bf_lo(bias2.y), bf_hi(bias2.y)};
    float r[4];
#pragma unroll
    for (int k = 0; k < 4; ++k) {
      const float hres = __shfl(v[k], NC + 32 * pr);
      const float zs = half_sum(c < NC ? v[k] : 0.f);
      r[k] = rbf(hres + rbf(rbf(zs) + bb[k]));
    }
    if (c == 0) *reinterpret_cast<uint2*>(out + col) = make_uint2(pack_bf(r[0], r[1]), pack_bf(r[2], r[3]));
    STAMP(0, 7);
    if (dj == 0 && tid == 0) __hip_atomic_store(eptr, epoch, __ATOMIC_RELAXED, __HIP_MEMORY_SCOPE_AGENT);
  }
}

extern "C" void kernel_launch(void* const* d_in, const int* in_sizes, int n_in, void* d_out, int out_size, void* d_ws, size_t ws_size,
                              hipStream_t stream) {
  const u16* x = (const u16*)d_in[0];
  const u16* kc = (const u16*)d_in[1];
  const u16* vc = (const u16*)d_in[2];
  const u16* wq = (const u16*)d_in[3];
  const u16* wo = (const u16*)d_in[4];
  const u16* w1 = (const u16*)d_in[6];
  const u16* b1 = (const u16*)d_in[7];
  const u16* w2 = (const u16*)d_in[8];
  const u16* b2 = (const u16*)d_in[9];
  k_fused<<<NWG, 512, 0, stream>>>(x, kc, vc, wq, wo, w1, b1, w2, b2, (u16*)d_out, (unsigned char*)d_ws);
}
```

```cpp
#include <hip/hip_runtime.h>
#include <hip/hip_bf16.h>
#include <stdint.h>

typedef unsigned short u16;
typedef unsigned int u32;
typedef unsigned long long u64;
typedef u32 u32x4 __attribute__((ext_vector_type(4)));
typedef float f32x4 __attribute__((ext_vector_type(4)));
typedef __bf16 bf16x8 __attribute__((ext_vector_type(8)));

constexpr int D_MODEL = 512, HQ = 32, HKV = 8, SK = 65536, DH = 128, FF = 1792;
constexpr int NSPLIT = 32;
constexpr int NWG = HKV * NSPLIT;
constexpr int NB = 32, NC = 28, ND = 8, NTAIL = NB + NC + ND;
constexpr int TILE_EL = 32 * DH;
constexpr float SCALE = 0.08838834764831845f;
constexpr float L2E = 1.4426950408889634f;
constexpr u32 SPIN_CAP = 1u << 18;
constexpr int AUX_SC1 = 16;

constexpr u32 WS_E = 0;
constexpr u32 WS_QG = 256;
constexpr u32 WS_PO = WS_QG + 4096 * 8;
constexpr u32 WS_PML = WS_PO + HQ * NSPLIT * 64 * 16;
constexpr u32 WS_Y = WS_PML + HQ * NSPLIT * 16;
constexpr u32 WS_Z = WS_Y + HQ * 256 * 16;
constexpr u32 WS_END = WS_Z + (NC + 1) * 256 * 16;

__device__ __forceinline__ float rbf(float x) { return (float)(__bf16)x; }
__device__ __forceinline__ float bf_lo(u32 w) { return __builtin_bit_cast(float, w << 16); }
__device__ __forceinline__ float bf_hi(u32 w) { return __builtin_bit_cast(float, w & 0xffff0000u); }
__device__ __forceinline__ float bf2f(u16 v) { return __builtin_bit_cast(float, (u32)v << 16); }
__device__ __forceinline__ u16 f2bf(float x) { return __builtin_bit_cast(u16, (__bf16)x); }
__device__ __forceinline__ u32 pack_bf(float lo, float hi) { return (u32)f2bf(lo) | ((u32)f2bf(hi) << 16); }
__device__ __forceinline__ u32 f2u(float x) { return __builtin_bit_cast(u32, x); }
__device__ __forceinline__ float u2f(u32 x) { return __builtin_bit_cast(float, x); }

__device__ __forceinline__ f32x4 mfma16(u32x4 a, u32x4 b, f32x4 c) {
  return __builtin_amdgcn_mfma_f32_16x16x32_bf16(__builtin_bit_cast(bf16x8, a), __builtin_bit_cast(bf16x8, b), c, 0, 0, 0);
}
template <int CTRL> __device__ __forceinline__ float dpp(float x) {
  return u2f((u32)__builtin_amdgcn_mov_dpp((int)f2u(x), CTRL, 0xf, 0xf, true));
}
constexpr int XOR1 = 0xB1, XOR2 = 0x4E, XOR7 = 0x141, XOR8 = 0x128;
__device__ __forceinline__ float x16_max(float x) {
  auto s = __builtin_amdgcn_permlane16_swap(f2u(x), f2u(x), false, false);
  return fmaxf(u2f(s[0]), u2f(s[1]));
}
__device__ __forceinline__ float x32_max(float x) {
  auto t = __builtin_amdgcn_permlane32_swap(f2u(x), f2u(x), false, false);
  return fmaxf(u2f(t[0]), u2f(t[1]));
}
__device__ __forceinline__ float x16_sum(float x) {
  auto s = __builtin_amdgcn_permlane16_swap(f2u(x), f2u(x), false, false);
  return u2f(s[0]) + u2f(s[1]);
}
__device__ __forceinline__ float x32_sum(float x) {
  auto t = __builtin_amdgcn_permlane32_swap(f2u(x), f2u(x), false, false);
  return u2f(t[0]) + u2f(t[1]);
}
__device__ __forceinline__ float row_sum(float x) {
  x += dpp<XOR1>(x); x += dpp<XOR2>(x); x += dpp<XOR7>(x); x += dpp<XOR8>(x);
  return x;
}
__device__ __forceinline__ float row_max(float x) {
  x = fmaxf(x, dpp<XOR1>(x)); x = fmaxf(x, dpp<XOR2>(x)); x = fmaxf(x, dpp<XOR7>(x)); x = fmaxf(x, dpp<XOR8>(x));
  return x;
}
__device__ __forceinline__ float half_sum(float x) { return x16_sum(row_sum(x)); }
__device__ __forceinline__ float half_max(float x) { return x16_max(row_max(x)); }
__device__ __forceinline__ float wave_sum(float x) { return x32_sum(half_sum(x)); }

__device__ __forceinline__ void wg_barrier() {
  asm volatile("s_waitcnt lgkmcnt(0)" ::: "memory");
  __builtin_amdgcn_s_barrier();
  asm volatile("" ::: "memory");
}
__device__ __forceinline__ u32x4 ld_sc1(__amdgpu_buffer_rsrc_t rs, u32 off) {
  return __builtin_amdgcn_raw_buffer_load_b128(rs, (int)off, 0, AUX_SC1);
}
__device__ __forceinline__ void st_sc1(__amdgpu_buffer_rsrc_t rs, u32 off, u32x4 v) {
  __builtin_amdgcn_raw_buffer_store_b128(v, rs, (int)off, 0, AUX_SC1);
}
__device__ __forceinline__ void glds16(const void* gsrc, u32 lds_dst) {
  u32 keep;
  asm volatile("s_mov_b32 %0, m0\n\ts_mov_b32 m0, %2\n\ts_nop 0\n\tglobal_load_lds_dwordx4 %1, off\n\ts_mov_b32 m0, %0"
               : "=&s"(keep) : "v"(gsrc), "s"(lds_dst) : "memory");
}

struct Tile { u32x4 k[8]; u32x4 v[8]; };

__device__ __forceinline__ void load_k(Tile& t, const u16* kp, int half) {
#pragma unroll
  for (int c = 0; c < 4; ++c)
    t.k[half * 4 + c] = __builtin_nontemporal_load(reinterpret_cast<const u32x4*>(kp + half * 16 * DH + c * 32));
  __builtin_amdgcn_sched_barrier(0);
}
__device__ __forceinline__ void load_v(Tile& t, const u16* vp, int half) {
#pragma unroll
  for (int i = 0; i < 4; ++i)
    t.v[half * 4 + i] = __builtin_nontemporal_load(reinterpret_cast<const u32x4*>(vp + (half * 16 + i) * DH));
  __builtin_amdgcn_sched_barrier(0);
}
__device__ __forceinline__ void load_tile(Tile& t, const u16* kp, const u16* vp) {
  load_k(t, kp, 0); load_k(t, kp, 1); load_v(t, vp, 0); load_v(t, vp, 1);
}

__device__ __forceinline__ void compute_tile(const Tile& t, const u32x4 (&qf)[4], float& m, float& l, f32x4 (&acc)[8]) {
  f32x4 s0 = {0.f, 0.f, 0.f, 0.f}, s1 = {0.f, 0.f, 0.f, 0.f};
#pragma unroll
  for (int c = 0; c < 4; ++c) {
    s0 = mfma16(t.k[c], qf[c], s0);
    s1 = mfma16(t.k[4 + c], qf[c], s1);
  }
  float sv[8];
#pragma unroll
  for (int r = 0; r < 4; ++r) {
    sv[r] = rbf(rbf(s0[r]) * SCALE);
    sv[4 + r] = rbf(rbf(s1[r]) * SCALE);
  }
  float tm = fmaxf(fmaxf(fmaxf(sv[0], sv[1]), fmaxf(sv[2], sv[3])), fmaxf(fmaxf(sv[4], sv[5]), fmaxf(sv[6], sv[7])));
  tm = x32_max(x16_max(tm));
  if (__any(tm > m)) {
    const float mn = fmaxf(m, tm);
    const float alpha = __builtin_amdgcn_exp2f((m - mn) * L2E);
    m = mn;
    l *= alpha;
#pragma unroll
    for (int j = 0; j < 8; ++j) acc[j] *= alpha;
  }
  float p[8];
#pragma unroll
  for (int i = 0; i < 8; ++i) {
    p[i] = __builtin_amdgcn_exp2f((sv[i] - m) * L2E);
    l += p[i];
  }
  u32x4 pf;
#pragma unroll
  for (int u = 0; u < 4; ++u) pf[u] = pack_bf(p[2 * u], p[2 * u + 1]);
#pragma unroll
  for (int w = 0; w < 4; ++w) {
    u32x4 lo, hi;
#pragma unroll
    for (int u = 0; u < 4; ++u) {
      lo[u] = __builtin_amdgcn_perm(t.v[2 * u + 1][w], t.v[2 * u][w], 0x05040100u);
      hi[u] = __builtin_amdgcn_perm(t.v[2 * u + 1][w], t.v[2 * u][w], 0x07060302u);
    }
    acc[2 * w] = mfma16(lo, pf, acc[2 * w]);
    acc[2 * w + 1] = mfma16(hi, pf, acc[2 * w + 1]);
  }
}

typedef __bf16 bf16x2 __attribute__((ext_vector_type(2)));
__device__ __forceinline__ float dot2(u32 w, u32 a, float c) {
  return __builtin_amdgcn_fdot2_f32_bf16(__builtin_bit_cast(bf16x2, w), __builtin_bit_cast(bf16x2, a), c, false);
}
__device__ __forceinline__ void dot8(float (&acc)[8], u32 alo, u32x4 w) {
  const u32 ahi = alo << 16;
#pragma unroll
  for (int k = 0; k < 4; ++k) {
    acc[2 * k] = dot2(w[k], alo, acc[2 * k]);
    acc[2 * k + 1] = dot2(w[k], ahi, acc[2 * k + 1]);
  }
}
__device__ __forceinline__ float tr32(float a, float b) {
  auto t = __builtin_amdgcn_permlane32_swap(f2u(a), f2u(b), false, false);
  return u2f(t[0]) + u2f(t[1]);
}
__device__ __forceinline__ float tr16(float a, float b) {
  auto t = __builtin_amdgcn_permlane16_swap(f2u(a), f2u(b), false, false);
  return u2f(t[0]) + u2f(t[1]);
}
__device__ __forceinline__ float treduce8_32(const float (&v)[8], int lane) {
  float c[4];
#pragma unroll
  for (int k = 0; k < 4; ++k) c[k] = tr16(v[k], v[k + 4]);
#pragma unroll
  for (int k = 0; k < 4; ++k) c[k] += dpp<XOR8>(c[k]);
  float d0 = (lane & 8) ? c[2] : c[0];
  float d1 = (lane & 8) ? c[3] : c[1];
  d0 += dpp<XOR7>(d0);
  d1 += dpp<XOR7>(d1);
  float e = (lane & 4) ? d1 : d0;
  e += dpp<XOR2>(e);
  e += dpp<XOR1>(e);
  return e;
}
__device__ __forceinline__ float treduce8_64(const float (&v)[8], int lane) {
  float c[4];
#pragma unroll
  for (int k = 0; k < 4; ++k) c[k] = tr32(v[k], v[k + 4]);
  float d0 = tr16(c[0], c[2]), d1 = tr16(c[1], c[3]);
  d0 += dpp<XOR8>(d0);
  d1 += dpp<XOR8>(d1);
  float e = (lane & 8) ? d1 : d0;
  e += dpp<XOR7>(e);
  e += dpp<XOR2>(e);
  e += dpp<XOR1>(e);
  return e;
}
__device__ __forceinline__ float div_rn(float a, float d, float rd) {
  const float q = a * rd;
  return fmaf(fmaf(-q, d, a), rd, q);
}

struct Smem {
  u32x4 wbuf[8192];
  float big[8 * 512];
  float yh[2 * 512];
  u32 lnb[512];
  float sm[8][4], sl[8][4];
  u32 qs[256];
  u64 epoch;
};

__global__ __launch_bounds__(512, 2) void k_fused(const u16* __restrict__ x, const u16* __restrict__ K, const u16* __restrict__ V,
                                                  const u16* __restrict__ wq, const u16* __restrict__ wo, const u16* __restrict__ w1,
                                                  const u16* __restrict__ b1, const u16* __restrict__ w2, const u16* __restrict__ b2,
                                                  u16* __restrict__ out, unsigned char* ws) {
  __shared__ __attribute__((aligned(16))) Smem sh;
  const int tid = threadIdx.x, lane = tid & 63, wave = __builtin_amdgcn_readfirstlane(tid >> 6);
  const int rho = lane & 15, g = lane >> 4;
  const int b = blockIdx.x, kvh = b & 7, sp = b >> 3;
  const __amdgpu_buffer_rsrc_t rs = __builtin_amdgcn_make_buffer_rsrc(ws, 0, (int)WS_END, 0x00020000);
  u64* eptr = reinterpret_cast<u64*>(ws + WS_E);

  const size_t base = ((size_t)kvh * SK + (size_t)sp * 2048 + wave * 32) * DH;
  const u16* kp = K + base + rho * DH + g * 8;
  const u16* vp = V + base + (4 * g) * DH + rho * 8;
  constexpr int TSTRIDE = 8 * TILE_EL;
  const int hb = (b & 7) * 4 + (b >> 3);
  const int cs = b - NB;

  Tile A, B;
  if (wave == 0) {
    const u32x4 zero4 = {0u, 0u, 0u, 0u};
#pragma unroll
    for (int h = 0; h < 4; ++h)
      st_sc1(rs, WS_PO + (u32)((((kvh * 4 + h) * 8 + (lane >> 3)) * NSPLIT + sp) * 8 + (lane & 7)) * 16, zero4);
    if (lane < 4) st_sc1(rs, WS_PML + (u32)((kvh * 4 + lane) * NSPLIT + sp) * 16, zero4);
    if (b < NB) {
#pragma unroll
      for (int i = 0; i < 4; ++i) st_sc1(rs, WS_Y + (u32)(hb * 256 + i * 64 + lane) * 16, zero4);
    } else if (b < NB + NC) {
#pragma unroll
      for (int i = 0; i < 4; ++i) st_sc1(rs, WS_Z + (u32)(cs * 256 + i * 64 + lane) * 16, zero4);
      if (b == NB) {
#pragma unroll
        for (int i = 0; i < 4; ++i) st_sc1(rs, WS_Z + (u32)(NC * 256 + i * 64 + lane) * 16, zero4);
      }
    }
    const u64 epoch = __hip_atomic_load(eptr, __ATOMIC_RELAXED, __HIP_MEMORY_SCOPE_AGENT) + 1ull;
    const int col0 = kvh * 512 + sp * 16;
    const u32x4 xv = *reinterpret_cast<const u32x4*>(x + lane * 8);
    u32x4 wv[16];
#pragma unroll
    for (int i = 0; i < 16; ++i)
      wv[i] = *reinterpret_cast<const u32x4*>(wq + (size_t)((lane & 31) + 32 * i) * 4096 + col0 + (lane >> 5) * 8);
    float xf[8];
#pragma unroll
    for (int k = 0; k < 4; ++k) { xf[2 * k] = bf_lo(xv[k]); xf[2 * k + 1] = bf_hi(xv[k]); }
    float s = 0.f;
#pragma unroll
    for (int k = 0; k < 8; ++k) s += xf[k];
    const float mean = wave_sum(s) * (1.0f / 512.0f);
    float s2 = 0.f;
#pragma unroll
    for (int k = 0; k < 8; ++k) s2 += (xf[k] - mean) * (xf[k] - mean);
    const float var = rbf(wave_sum(s2) * (1.0f / 512.0f));
    const float mb = rbf(mean);
    const float den = rbf(sqrtf(rbf(var + 1e-5f)));
    const float rden = 1.0f / den;
#pragma unroll
    for (int k = 0; k < 8; ++k) sh.lnb[lane * 8 + k] = (u32)f2bf(div_rn(rbf(xf[k] - mb), den, rden));
    asm volatile("s_waitcnt lgkmcnt(0)" ::: "memory");
    float acc[8];
#pragma unroll
    for (int k = 0; k < 8; ++k) acc[k] = 0.f;
#pragma unroll
    for (int i = 0; i < 16; ++i) dot8(acc, sh.lnb[(lane & 31) + 32 * i], wv[i]);
    const float qv1 = treduce8_32(acc, lane);
    const u32 tlo = (u32)(epoch << 16), thi = (u32)(epoch >> 16);
    asm volatile("s_waitcnt vmcnt(0)" ::: "memory");
    if ((lane & 3) == 0)
      __hip_atomic_store(reinterpret_cast<u64*>(ws + WS_QG) + col0 + (lane >> 2), ((u64)thi << 32) | (u64)(tlo | (u32)f2bf(qv1)),
                         __ATOMIC_RELAXED, __HIP_MEMORY_SCOPE_AGENT);
    __builtin_amdgcn_s_barrier();
    const bool tailwg = b < NTAIL;
    const u32 toff0 = WS_QG + (u32)((lane & 7) * 512 + (lane >> 3) * 16) * 8;
    const int b2i = 64 + (lane & 3);
    const u32 toff1 = WS_QG + (u32)((b2i & 7) * 512 + (b2i >> 3) * 16) * 8;
    u32x4 qv[4];
    for (u32 spin = 0;; ++spin) {
      asm volatile("" ::: "memory");
      bool ok = true;
#pragma unroll
      for (int j = 0; j < 4; ++j) qv[j] = ld_sc1(rs, WS_QG + (u32)(kvh * 512) * 8 + (u32)(lane + 64 * j) * 16);
      if (tailwg) {
        const u32x4 t0v = ld_sc1(rs, toff0), t1v = ld_sc1(rs, toff1);
        ok = ((t0v[0] & 0xffff0000u) == tlo) && (t0v[1] == thi) && ((t1v[0] & 0xffff0000u) == tlo) && (t1v[1] == thi);
      }
#pragma unroll
      for (int j = 0; j < 4; ++j)
        ok = ok && ((qv[j][0] & 0xffff0000u) == tlo) && (qv[j][1] == thi) && ((qv[j][2] & 0xffff0000u) == tlo) && (qv[j][3] == thi);
      if (__all(ok) || spin > SPIN_CAP) break;
      __builtin_amdgcn_s_sleep(1);
    }
#pragma unroll
    for (int j = 0; j < 4; ++j) sh.qs[lane + 64 * j] = (qv[j][0] & 0xffffu) | (qv[j][2] << 16);
    if (lane == 0) sh.epoch = epoch;
  } else {
    const u32 wbase = (u32)(size_t)(__attribute__((address_space(3))) void*)sh.wbuf;
    __builtin_amdgcn_s_sleep(2);
    if (b < NB) {
      const u16* src = wo + (size_t)hb * DH * D_MODEL + lane * 8;
      for (int p = wave - 1; p < 128; p += 7) glds16(src + (size_t)p * 512, wbase + (u32)p * 1024);
    } else if (b < NB + NC) {
      const u16* s1 = w1 + (size_t)(lane >> 3) * FF + cs * 64 + (((lane & 7) ^ (lane >> 3)) & 7) * 8;
      const u16* s2 = w2 + (size_t)cs * 64 * D_MODEL + lane * 8;
      for (int p = wave - 1; p < 64; p += 7) {
        glds16(s1 + (size_t)p * 8 * FF, wbase + (u32)p * 1024);
        glds16(s2 + (size_t)p * 512, wbase + 65536u + (u32)p * 1024);
      }
    }
    __builtin_amdgcn_s_barrier();
  }
  load_tile(A, kp, vp);
  load_tile(B, kp + TSTRIDE, vp + TSTRIDE);
  wg_barrier();
  const u64 epoch = sh.epoch;
  const u32 tag = (u32)epoch * 2u + 1u;

  u32x4 qf[4];
#pragma unroll
  for (int c = 0; c < 4; ++c) {
    qf[c] = u32x4{0u, 0u, 0u, 0u};
    if (rho < 4) qf[c] = *reinterpret_cast<const u32x4*>(&sh.qs[rho * 64 + c * 16 + g * 4]);
  }
  float m = -INFINITY, l = 0.f;
  f32x4 acc[8];
#pragma unroll
  for (int j = 0; j < 8; ++j) acc[j] = f32x4{0.f, 0.f, 0.f, 0.f};

#pragma unroll
  for (int it = 0; it < 6; it += 2) {
    compute_tile(A, qf, m, l, acc);
    load_tile(A, kp + (size_t)(it + 2) * TSTRIDE, vp + (size_t)(it + 2) * TSTRIDE);
    compute_tile(B, qf, m, l, acc);
    load_tile(B, kp + (size_t)(it + 3) * TSTRIDE, vp + (size_t)(it + 3) * TSTRIDE);
  }
  compute_tile(A, qf, m, l, acc);
  compute_tile(B, qf, m, l, acc);

  l = x32_sum(x16_sum(l));
  if (rho < 4) {
#pragma unroll
    for (int r = 0; r < 4; ++r) {
      f32x4 a = {acc[0][r], acc[1][r], acc[2][r], acc[3][r]};
      f32x4 c = {acc[4][r], acc[5][r], acc[6][r], acc[7][r]};
      float* dst = &sh.big[(wave * 4 + rho) * DH + 32 * g + 8 * r];
      *reinterpret_cast<f32x4*>(dst) = a;
      *reinterpret_cast<f32x4*>(dst + 4) = c;
    }
    if (g == 0) { sh.sm[wave][rho] = m; sh.sl[wave][rho] = l; }
  }
  asm volatile("s_waitcnt vmcnt(0)" ::: "memory");
  wg_barrier();
  if (tid < 256) {
    const int h = tid >> 6, dp = tid & 63;
    float M = sh.sm[0][h];
#pragma unroll
    for (int w = 1; w < 8; ++w) M = fmaxf(M, sh.sm[w][h]);
    float L = 0.f, O0 = 0.f, O1 = 0.f;
#pragma unroll
    for (int w = 0; w < 8; ++w) {
      const float e = __builtin_amdgcn_exp2f((sh.sm[w][h] - M) * L2E);
      L += e * sh.sl[w][h];
      O0 += e * sh.big[(w * 4 + h) * DH + 2 * dp];
      O1 += e * sh.big[(w * 4 + h) * DH + 2 * dp + 1];
    }
    st_sc1(rs, WS_PO + (u32)((((kvh * 4 + h) * 8 + (dp >> 3)) * NSPLIT + sp) * 8 + (dp & 7)) * 16, u32x4{f2u(O0), tag, f2u(O1), tag});
    if (dp == 0) st_sc1(rs, WS_PML + (u32)((kvh * 4 + h) * NSPLIT + sp) * 16, u32x4{f2u(M), tag, f2u(L), tag});
  }
  if (b >= NTAIL) return;
  wg_barrier();

  if (b < NB) {
    const int s = lane & 31, hf = lane >> 5;
    u32x4 ml, o4[4];
    for (u32 spin = 0;; ++spin) {
      asm volatile("" ::: "memory");
      ml = ld_sc1(rs, WS_PML + (u32)(hb * NSPLIT + s) * 16);
#pragma unroll
      for (int j = 0; j < 4; ++j) o4[j] = ld_sc1(rs, WS_PO + (u32)(((hb * 8 + wave) * NSPLIT + s) * 8 + hf * 4 + j) * 16);
      bool ok = ml[1] == tag && ml[3] == tag;
#pragma unroll
      for (int j = 0; j < 4; ++j) ok = ok && o4[j][1] == tag && o4[j][3] == tag;
      if (__all(ok) || spin > SPIN_CAP) break;
      __builtin_amdgcn_s_sleep(1);
    }
    const float ms = u2f(ml[0]);
    const float M = half_max(ms);
    const float wgt = __builtin_amdgcn_exp2f((ms - M) * L2E);
    const float L = half_sum(wgt * u2f(ml[2]));
    float ow[8];
#pragma unroll
    for (int j = 0; j < 4; ++j) { ow[2 * j] = wgt * u2f(o4[j][0]); ow[2 * j + 1] = wgt * u2f(o4[j][2]); }
    const u32 ob = (u32)f2bf(treduce8_32(ow, lane) / L);
    float a8[8];
#pragma unroll
    for (int k = 0; k < 8; ++k) a8[k] = 0.f;
#pragma unroll
    for (int i = 0; i < 16; ++i) dot8(a8, (u32)__builtin_amdgcn_readlane((int)ob, i * 4), sh.wbuf[(wave * 16 + i) * 64 + lane]);
    *reinterpret_cast<f32x4*>(&sh.big[wave * 512 + lane * 8]) = f32x4{a8[0], a8[1], a8[2], a8[3]};
    *reinterpret_cast<f32x4*>(&sh.big[wave * 512 + lane * 8 + 4]) = f32x4{a8[4], a8[5], a8[6], a8[7]};
    wg_barrier();
    if (tid < 256) {
      float y0 = 0.f, y1 = 0.f;
#pragma unroll
      for (int w = 0; w < 8; ++w) { y0 += sh.big[w * 512 + 2 * tid]; y1 += sh.big[w * 512 + 2 * tid + 1]; }
      st_sc1(rs, WS_Y + (u32)(hb * 256 + tid) * 16, u32x4{f2u(y0), tag, f2u(y1), tag});
    }
  } else if (b < NB + NC) {
    const int cp = tid & 255, hh = tid >> 8;
    float xres[8];
#pragma unroll
    for (int i = 0; i < 8; ++i) xres[i] = bf2f(x[lane + 64 * i]);
    const float bias1 = bf2f(b1[cs * 64 + wave * 8 + (lane >> 3)]);
    u32x4 yv[16];
    for (u32 spin = 0;; ++spin) {
      asm volatile("" ::: "memory");
#pragma unroll
      for (int j = 0; j < 16; ++j) yv[j] = ld_sc1(rs, WS_Y + (u32)((hh * 16 + j) * 256 + cp) * 16);
      bool ok = true;
#pragma unroll
      for (int j = 0; j < 16; ++j) ok = ok && yv[j][1] == tag && yv[j][3] == tag;
      if (__all(ok) || spin > SPIN_CAP) break;
      __builtin_amdgcn_s_sleep(1);
    }
    float s0 = 0.f, s1 = 0.f;
#pragma unroll
    for (int j = 0; j < 16; ++j) { s0 += u2f(yv[j][0]); s1 += u2f(yv[j][2]); }
    *reinterpret_cast<float2*>(&sh.yh[hh * 512 + 2 * cp]) = make_float2(s0, s1);
    wg_barrier();
    float hv[8], hs = 0.f;
#pragma unroll
    for (int i = 0; i < 8; ++i) {
      hv[i] = rbf(xres[i] + rbf(sh.yh[lane + 64 * i] + sh.yh[512 + lane + 64 * i]));
      hs += hv[i];
    }
    if (cs == 0 && wave < 4) {
      const int c0 = wave * 128 + 2 * lane;
      const float h0 = rbf(bf2f(x[c0]) + rbf(sh.yh[c0] + sh.yh[512 + c0]));
      const float h1 = rbf(bf2f(x[c0 + 1]) + rbf(sh.yh[c0 + 1] + sh.yh[512 + c0 + 1]));
      st_sc1(rs, WS_Z + (u32)(NC * 256 + tid) * 16, u32x4{f2u(h0), tag, f2u(h1), tag});
    }
    const float mean = wave_sum(hs) * (1.0f / 512.0f);
    float vs = 0.f;
#pragma unroll
    for (int i = 0; i < 8; ++i) vs += (hv[i] - mean) * (hv[i] - mean);
    const float var = rbf(wave_sum(vs) * (1.0f / 512.0f));
    const float mb = rbf(mean);
    const float den = rbf(sqrtf(rbf(var + 1e-5f)));
    const float rden = 1.0f / den;
    float a8[8];
#pragma unroll
    for (int k = 0; k < 8; ++k) a8[k] = 0.f;
#pragma unroll
    for (int i = 0; i < 8; ++i)
      dot8(a8, (u32)f2bf(div_rn(rbf(hv[i] - mb), den, rden)), sh.wbuf[(lane + 64 * i) * 8 + ((wave ^ lane) & 7)]);
    const float h1 = rbf(rbf(treduce8_64(a8, lane)) + bias1);
    const float rr = h1 > 0.f ? h1 : 0.f;
    const u32 hb16 = (u32)f2bf(rr * rr);
    float z8[8];
#pragma unroll
    for (int k = 0; k < 8; ++k) z8[k] = 0.f;
#pragma unroll
    for (int k = 0; k < 8; ++k) dot8(z8, (u32)__builtin_amdgcn_readlane((int)hb16, k * 8), sh.wbuf[4096 + (wave * 8 + k) * 64 + lane]);
    *reinterpret_cast<f32x4*>(&sh.big[wave * 512 + lane * 8]) = f32x4{z8[0], z8[1], z8[2], z8[3]};
    *reinterpret_cast<f32x4*>(&sh.big[wave * 512 + lane * 8 + 4]) = f32x4{z8[4], z8[5], z8[6], z8[7]};
    wg_barrier();
    if (tid < 256) {
      float z0 = 0.f, z1 = 0.f;
#pragma unroll
      for (int w = 0; w < 8; ++w) { z0 += sh.big[w * 512 + 2 * tid]; z1 += sh.big[w * 512 + 2 * tid + 1]; }
      st_sc1(rs, WS_Z + (u32)(cs * 256 + tid) * 16, u32x4{f2u(z0), tag, f2u(z1), tag});
    }
  } else {
    const int dj = b - NB - NC;
    const int c = lane & 31, pr = lane >> 5;
    const int cc = c <= NC ? c : NC;
    const int col = dj * 64 + wave * 8 + pr * 4;
    const uint2 bias2 = *reinterpret_cast<const uint2*>(b2 + col);
    u32x4 z0, z1;
    for (u32 spin = 0;; ++spin) {
      asm volatile("" ::: "memory");
      z0 = ld_sc1(rs, WS_Z + (u32)(cc * 256 + (col >> 1)) * 16);
      z1 = ld_sc1(rs, WS_Z + (u32)(cc * 256 + (col >> 1) + 1) * 16);
      const bool ok = z0[1] == tag && z0[3] == tag && z1[1] == tag && z1[3] == tag;
      if (__all(ok) || spin > SPIN_CAP) break;
      __builtin_amdgcn_s_sleep(1);
    }
    float v[4] = {u2f(z0[0]), u2f(z0[2]), u2f(z1[0]), u2f(z1[2])};
    const float bb[4] = {bf_lo(bias2.x), bf_hi(bias2.x), bf_lo(bias2.y), bf_hi(bias2.y)};
    float r[4];
#pragma unroll
    for (int k = 0; k < 4; ++k) {
      const float hres = __shfl(v[k], NC + 32 * pr);
      const float zs = half_sum(c < NC ? v[k] : 0.f);
      r[k] = rbf(hres + rbf(rbf(zs) + bb[k]));
    }
    if (c == 0) *reinterpret_cast<uint2*>(out + col) = make_uint2(pack_bf(r[0], r[1]), pack_bf(r[2], r[3]));
    if (dj == 0 && tid == 0) __hip_atomic_store(eptr, epoch, __ATOMIC_RELAXED, __HIP_MEMORY_SCOPE_AGENT);
  }
}

extern "C" void kernel_launch(void* const* d_in, const int* in_sizes, int n_in, void* d_out, int out_size, void* d_ws, size_t ws_size,
                              hipStream_t stream) {
  const u16* x = (const u16*)d_in[0];
  const u16* kc = (const u16*)d_in[1];
  const u16* vc = (const u16*)d_in[2];
  const u16* wq = (const u16*)d_in[3];
  const u16* wo = (const u16*)d_in[4];
  const u16* w1 = (const u16*)d_in[6];
  const u16* b1 = (const u16*)d_in[7];
  const u16* w2 = (const u16*)d_in[8];
  const u16* b2 = (const u16*)d_in[9];
  k_fused<<<NWG, 512, 0, stream>>>(x, kc, vc, wq, wo, w1, b1, w2, b2, (u16*)d_out, (unsigned char*)d_ws);
}
```
